```python
import torch

def _softmax(x, axis=-1):
    return torch.softmax(x, dim=axis)
def _gelu(x):
    return torch.nn.functional.gelu(x, approximate="tanh")
def _silu(x):
    return torch.nn.functional.silu(x)
def _sigmoid(x):
    return torch.sigmoid(x)
def _layernorm(x, axis=-1, eps=1e-5):
    m = x.mean(dim=axis, keepdim=True)
    v = x.var(dim=axis, keepdim=True, unbiased=False)
    return (x - m) / torch.sqrt(v + eps)
def _scatter_add(tgt, idx, src, axis=0):
    return tgt.clone().index_add_(axis, idx, src)
def _segment_sum(src, seg, n):
    out = torch.zeros((n,) + src.shape[1:], dtype=src.dtype)
    return out.index_add_(0, seg, src)
def _pad(x, pw):
    flat = tuple(v for pair in reversed(pw) for v in pair)
    return torch.nn.functional.pad(x, flat)

kernel_name = 'concat_heads__sliding_block_cached__gqa_block_cached__decode'

def setup_inputs():

    _w = (lambda a: (lambda t: t.to(torch.bfloat16) if t.is_floating_point() else t)(torch.as_tensor(a)))
    def _A_setup():
        B, Sq, D = 1, 1, 640
        Hq, Hkv, Sk, Dh = 32, 32, 4096, 128
        assert Hq % Hkv == 0, (Hq, Hkv)
        return {
            "x": _w((lambda *s: torch.randn(*s, dtype=torch.bfloat16) * 0.2)(B, Sq, D)),
            "k_cache": _w((lambda *s: torch.randn(*s, dtype=torch.bfloat16) * 3.2)(B, Hkv, Sk, Dh)),
            "v_cache": _w((lambda *s: torch.randn(*s, dtype=torch.bfloat16) * 1.6)(B, Hkv, Sk, Dh)),
            "wq": _w((lambda *s: torch.randn(*s, dtype=torch.bfloat16) * 3.2)(D, Hq * Dh) * (D ** -0.5)),
            "wo": _w((lambda *s: torch.randn(*s, dtype=torch.bfloat16) * 1.6)(Hq * Dh, D) * ((Hq * Dh) ** -0.5)),
        }
    _A_in = _A_setup()

    def _B_setup():
        B, Sq, D = 1, 1, 640
        Hq, Hkv, Sk, Dh = 32, 32, 4096, 128
        assert Hq % Hkv == 0, (Hq, Hkv)
        return {
            "x": _w((lambda *s: torch.randn(*s, dtype=torch.bfloat16) * 0.4)(B, Sq, D)),
            "k_cache": _w((lambda *s: torch.randn(*s, dtype=torch.bfloat16) * 3.2)(B, Hkv, Sk, Dh)),
            "v_cache": _w((lambda *s: torch.randn(*s, dtype=torch.bfloat16) * 0.8)(B, Hkv, Sk, Dh)),
            "wq": _w((lambda *s: torch.randn(*s, dtype=torch.bfloat16) * 3.2)(D, Hq * Dh) * (D ** -0.5)),
            "wo": _w((lambda *s: torch.randn(*s, dtype=torch.bfloat16) * 0.8)(Hq * Dh, D) * ((Hq * Dh) ** -0.5)),
        }
    _B_in = _B_setup()

    return {
        "A_x": _A_in["x"],
        "A_k_cache": _A_in["k_cache"],
        "A_v_cache": _A_in["v_cache"],
        "A_wq": _A_in["wq"],
        "A_wo": _A_in["wo"],
        "B_x": _B_in["x"],
        "B_k_cache": _B_in["k_cache"],
        "B_v_cache": _B_in["v_cache"],
        "B_wq": _B_in["wq"],
        "B_wo": _B_in["wo"],
    }

def reference(A_x, A_k_cache, A_v_cache, A_wq, A_wo, B_x, B_k_cache, B_v_cache, B_wq, B_wo):
    def _A_ref(x, k_cache, v_cache, wq, wo):
        B, Sq, D = x.shape
        Hq, Dh = 32, 128
        Hkv, Sk = k_cache.shape[1], k_cache.shape[2]
        W = min(4096, max(256, Sk // 4))
        rep = Hq // Hkv
        q = (lambda x, *d: x.permute(*d))((x @ wq).reshape(B, Sq, Hq, Dh), 0, 2, 1, 3)
        k = (lambda x, n, axis: torch.repeat_interleave(x, n, dim=axis))(k_cache[:, :, Sk - W:, :], rep, axis=1)
        v = (lambda x, n, axis: torch.repeat_interleave(x, n, dim=axis))(v_cache[:, :, Sk - W:, :], rep, axis=1)
        scores = torch.einsum("bhqd,bhkd->bhqk", q, k) * (Dh ** -0.5)
        p = _softmax(scores, axis=-1)
        o = torch.einsum("bhqk,bhkd->bhqd", p, v)
        return (lambda x, *d: x.permute(*d))(o, 0, 2, 1, 3).reshape(B, Sq, Hq * Dh) @ wo

    def _B_ref(x, k_cache, v_cache, wq, wo):
        B, Sq, D = x.shape
        Hq, Dh = 32, 128
        Hkv = k_cache.shape[1]
        rep = Hq // Hkv
        q = (lambda x, *d: x.permute(*d))((x @ wq).reshape(B, Sq, Hq, Dh), 0, 2, 1, 3)
        k = (lambda x, n, axis: torch.repeat_interleave(x, n, dim=axis))(k_cache, rep, axis=1)
        v = (lambda x, n, axis: torch.repeat_interleave(x, n, dim=axis))(v_cache, rep, axis=1)
        scores = torch.einsum("bhqd,bhkd->bhqk", q, k) * (Dh ** -0.5)
        p = _softmax(scores, axis=-1)
        o = torch.einsum("bhqk,bhkd->bhqd", p, v)
        return (lambda x, *d: x.permute(*d))(o, 0, 2, 1, 3).reshape(B, Sq, Hq * Dh) @ wo

    a = _A_ref(A_x, A_k_cache, A_v_cache, A_wq, A_wo)
    b = _B_ref(B_x, B_k_cache, B_v_cache, B_wq, B_wo)
    return torch.concatenate([a, b], axis=-1)
```

```cpp
#include <hip/hip_runtime.h>
#include <hip/hip_bf16.h>
#include <stdint.h>

#define MIMIC 1

typedef unsigned u32x4 __attribute__((ext_vector_type(4)));
typedef float f32x4 __attribute__((ext_vector_type(4)));

constexpr int DM = 640, H = 32, SK = 4096, DH = 128, WIN = 1024, HD = H * DH;
constexpr int CH = 128;
constexpr int NSA = WIN / CH, NSB = SK / CH;

__device__ __forceinline__ float bflo(unsigned u) { return __uint_as_float(u << 16); }
__device__ __forceinline__ float bfhi(unsigned u) { return __uint_as_float(u & 0xffff0000u); }
__device__ __forceinline__ float rbf(float f) { return __bfloat162float(__float2bfloat16(f)); }

__global__ void __launch_bounds__(256) k_q(const uint16_t* xA, const uint16_t* wqA, const uint16_t* xB, const uint16_t* wqB, float* q) {
  __shared__ float red[16][128];
  const int br = blockIdx.x >> 5, h = blockIdx.x & 31;
  const uint16_t* x = br ? xB : xA;
  const uint16_t* wq = br ? wqB : wqA;
  const int t = threadIdx.x, c = t & 15, rg = t >> 4;
  float acc[8];
#pragma unroll
  for (int i = 0; i < 8; ++i) acc[i] = 0.f;
  for (int r = rg; r < DM; r += 16) {
    const float xv = __uint_as_float((unsigned)x[r] << 16);
    const u32x4 w = *reinterpret_cast<const u32x4*>(wq + (size_t)r * HD + h * DH + c * 8);
#pragma unroll
    for (int i = 0; i < 4; ++i) { acc[2 * i] += xv * bflo(w[i]); acc[2 * i + 1] += xv * bfhi(w[i]); }
  }
#pragma unroll
  for (int i = 0; i < 8; ++i) red[rg][c * 8 + i] = acc[i];
  __syncthreads();
  if (t < 128) {
    float s = 0.f;
    for (int g = 0; g < 16; ++g) s += red[g][t];
    q[br * HD + h * DH + t] = rbf(s);
  }
}

__global__ void __launch_bounds__(256) k_attn(const uint16_t* kA, const uint16_t* vA, const uint16_t* kB, const uint16_t* vB,
                                              const float* q, float* pml, float* po) {
  __shared__ float sc[CH];
  __shared__ float red[16][128];
  __shared__ float stat[2];
  int b = blockIdx.x;
  int br, h, s;
  if (b < H * NSB) { br = 1; h = b / NSB; s = b % NSB; } else { b -= H * NSB; br = 0; h = b / NSA; s = b % NSA; }
  const int key0 = br ? s * CH : (SK - WIN) + s * CH;
  const uint16_t* K = (br ? kB : kA) + ((size_t)h * SK + key0) * DH;
  const uint16_t* V = (br ? vB : vA) + ((size_t)h * SK + key0) * DH;
  const int t = threadIdx.x, c = t & 15, rg = t >> 4;
  float qv[8];
#pragma unroll
  for (int i = 0; i < 8; ++i) qv[i] = q[br * HD + h * DH + c * 8 + i];
  for (int r = rg; r < CH; r += 16) {
    const u32x4 w = __builtin_nontemporal_load(reinterpret_cast<const u32x4*>(K + (size_t)r * DH + c * 8));
    float d = 0.f;
#pragma unroll
    for (int i = 0; i < 4; ++i) { d += qv[2 * i] * bflo(w[i]); d += qv[2 * i + 1] * bfhi(w[i]); }
    d += __shfl_xor(d, 1); d += __shfl_xor(d, 2); d += __shfl_xor(d, 4); d += __shfl_xor(d, 8);
    d = rbf(rbf(d) * 0.08838834764831845f);
    if (c == 0) sc[r] = d;
  }
  __syncthreads();
  if (t < 64) {
    float a = sc[t], bb = sc[t + 64];
    float m = fmaxf(a, bb);
    for (int o = 32; o > 0; o >>= 1) m = fmaxf(m, __shfl_xor(m, o));
    a = __expf(a - m); bb = __expf(bb - m);
    sc[t] = a; sc[t + 64] = bb;
    float l = a + bb;
    for (int o = 32; o > 0; o >>= 1) l += __shfl_xor(l, o);
    if (t == 0) { stat[0] = m; stat[1] = l; }
  }
  __syncthreads();
  float acc[8];
#pragma unroll
  for (int i = 0; i < 8; ++i) acc[i] = 0.f;
  for (int r = rg; r < CH; r += 16) {
    const float p = sc[r];
    const u32x4 w = __builtin_nontemporal_load(reinterpret_cast<const u32x4*>(V + (size_t)r * DH + c * 8));
#pragma unroll
    for (int i = 0; i < 4; ++i) { acc[2 * i] += p * bflo(w[i]); acc[2 * i + 1] += p * bfhi(w[i]); }
  }
#pragma unroll
  for (int i = 0; i < 8; ++i) red[rg][c * 8 + i] = acc[i];
  __syncthreads();
  const int slot = (br * H + h) * 32 + s;
  if (t < 128) {
    float sum = 0.f;
    for (int g = 0; g < 16; ++g) sum += red[g][t];
    po[(size_t)slot * DH + t] = sum;
  }
  if (t == 0) { pml[slot * 2] = stat[0]; pml[slot * 2 + 1] = stat[1]; }
}

__global__ void __launch_bounds__(320) k_o(const uint16_t* woA, const uint16_t* woB, const float* pml, const float* po, float* y) {
  __shared__ float o[DH];
  __shared__ float red[4][640];
  const int br = blockIdx.x >> 5, h = blockIdx.x & 31;
  const int ns = br ? NSB : NSA;
  const int t = threadIdx.x;
  const int base = (br * H + h) * 32;
  if (t < 128) {
    float M = -INFINITY;
    for (int s = 0; s < ns; ++s) M = fmaxf(M, pml[(base + s) * 2]);
    float L = 0.f, acc = 0.f;
    for (int s = 0; s < ns; ++s) {
      const float w = __expf(pml[(base + s) * 2] - M);
      L += w * pml[(base + s) * 2 + 1];
      acc += w * po[(size_t)(base + s) * DH + t];
    }
    float r = acc / L;
    r = rbf(r);
    o[t] = r;
  }
  __syncthreads();
  const uint16_t* wo = (br ? woB : woA) + (size_t)h * DH * DM;
  const int c = t % 80, rg = t / 80;
  float acc[8];
#pragma unroll
  for (int i = 0; i < 8; ++i) acc[i] = 0.f;
  for (int r = rg; r < DH; r += 4) {
    const float p = o[r];
    const u32x4 w = *reinterpret_cast<const u32x4*>(wo + (size_t)r * DM + c * 8);
#pragma unroll
    for (int i = 0; i < 4; ++i) { acc[2 * i] += p * bflo(w[i]); acc[2 * i + 1] += p * bfhi(w[i]); }
  }
#pragma unroll
  for (int i = 0; i < 8; ++i) red[rg][c * 8 + i] = acc[i];
  __syncthreads();
  for (int n = t; n < DM; n += 320) y[(size_t)(br * H + h) * DM + n] = red[0][n] + red[1][n] + red[2][n] + red[3][n];
}

__global__ void __launch_bounds__(256) k_fin(const float* y, __hip_bfloat16* out) {
  const int n = blockIdx.x * 256 + threadIdx.x;
  if (n >= 2 * DM) return;
  const int br = n / DM, c = n % DM;
  float s = 0.f;
  for (int h = 0; h < H; ++h) s += y[(size_t)(br * H + h) * DM + c];
  out[n] = __float2bfloat16(s);
}

extern "C" void kernel_launch(void* const* d_in, const int* in_sizes, int n_in, void* d_out, int out_size, void* d_ws, size_t ws_size,
                              hipStream_t stream) {
  const uint16_t* xA = (const uint16_t*)d_in[0];
  const uint16_t* kA = (const uint16_t*)d_in[1];
  const uint16_t* vA = (const uint16_t*)d_in[2];
  const uint16_t* wqA = (const uint16_t*)d_in[3];
  const uint16_t* woA = (const uint16_t*)d_in[4];
  const uint16_t* xB = (const uint16_t*)d_in[5];
  const uint16_t* kB = (const uint16_t*)d_in[6];
  const uint16_t* vB = (const uint16_t*)d_in[7];
  const uint16_t* wqB = (const uint16_t*)d_in[8];
  const uint16_t* woB = (const uint16_t*)d_in[9];
  float* ws = (float*)d_ws;
  float* q = ws;
  float* pml = ws + 8192;
  float* po = ws + 16384;
  float* y = ws + 16384 + 262144;
  k_q<<<64, 256, 0, stream>>>(xA, wqA, xB, wqB, q);
  k_attn<<<H * (NSA + NSB), 256, 0, stream>>>(kA, vA, kB, vB, q, pml, po);
  k_o<<<64, 320, 0, stream>>>(woA, woB, pml, po, y);
  k_fin<<<5, 256, 0, stream>>>(y, (__hip_bfloat16*)d_out);
}
```

```cpp
#include <hip/hip_runtime.h>
#include <stdint.h>

#define AUX_KV 2
#define AUX_WQ 2
#define AUX_WO 2
#define STAMPS 0

typedef unsigned v4u __attribute__((ext_vector_type(4)));
typedef unsigned v2u __attribute__((ext_vector_type(2)));
typedef float f32x4 __attribute__((ext_vector_type(4)));
typedef float f32x2 __attribute__((ext_vector_type(2)));
typedef __bf16 bf16x8 __attribute__((ext_vector_type(8)));
typedef __bf16 bf16x2 __attribute__((ext_vector_type(2)));
typedef short s16x4 __attribute__((ext_vector_type(4)));
#define LAS __attribute__((address_space(3)))

constexpr int DM = 640, NH = 32, SK = 4096, DH = 128, WIN = 1024, HD = NH * DH;
constexpr int AUX_SC1 = 16;
constexpr unsigned RSRC_W3 = 0x00020000u;
constexpr int WS_E = 0;
constexpr int WS_ML = 4096;
constexpr int WS_PO = 16384;
constexpr int WS_Y = WS_PO + 32 * 8 * 1024;
constexpr int WS_END = WS_Y + 2 * 10 * 32 * 512;
constexpr int WS_STAMP = 1 << 20;
constexpr unsigned SPIN_CAP = 1u << 16;
constexpr int L_QP = 0;
constexpr int L_OP = 4096;
constexpr int L_XB = 8192;
constexpr int L_WM = 10240;
constexpr int L_WL = 10272;
constexpr int L_END = 10304;

struct Params {
  const unsigned char* x[2];
  const unsigned char* k[2];
  const unsigned char* v[2];
  const unsigned char* wq[2];
  const unsigned char* wo[2];
  unsigned char* out;
  unsigned char* ws;
};

__device__ __forceinline__ __amdgpu_buffer_rsrc_t mk(const unsigned char* p, unsigned bytes) {
  return __builtin_amdgcn_make_buffer_rsrc((void*)p, 0, (int)bytes, RSRC_W3);
}
template <int AUX> __device__ __forceinline__ v4u ld128(__amdgpu_buffer_rsrc_t rs, int voff, int soff) {
  return __builtin_amdgcn_raw_buffer_load_b128(rs, voff, soff, AUX);
}
__device__ __forceinline__ f32x4 mfma32(v4u a, v4u b, f32x4 c) {
  return __builtin_amdgcn_mfma_f32_16x16x32_bf16(__builtin_bit_cast(bf16x8, a), __builtin_bit_cast(bf16x8, b), c, 0, 0, 0);
}
__device__ __forceinline__ f32x4 mfma16(v2u a, v2u b, f32x4 c) {
  return __builtin_amdgcn_mfma_f32_16x16x16bf16_1k(__builtin_bit_cast(s16x4, a), __builtin_bit_cast(s16x4, b), c, 0, 0, 0);
}
__device__ __forceinline__ unsigned pk_lo(unsigned r0, unsigned r1) { return __builtin_amdgcn_perm(r1, r0, 0x05040100u); }
__device__ __forceinline__ unsigned pk_hi(unsigned r0, unsigned r1) { return __builtin_amdgcn_perm(r1, r0, 0x07060302u); }
__device__ __forceinline__ unsigned cvtpk(float lo, float hi) {
  f32x2 f = {lo, hi};
  return __builtin_bit_cast(unsigned, __builtin_convertvector(f, bf16x2));
}
__device__ __forceinline__ float bflo(unsigned u) { return __builtin_bit_cast(float, u << 16); }
__device__ __forceinline__ float bfhi(unsigned u) { return __builtin_bit_cast(float, u & 0xffff0000u); }
template <int CTRL> __device__ __forceinline__ float dpp(float x) {
  return __builtin_bit_cast(float, __builtin_amdgcn_mov_dpp(__builtin_bit_cast(int, x), CTRL, 0xf, 0xf, true));
}
__device__ __forceinline__ float xrow_max(float x) {
  auto s = __builtin_amdgcn_permlane16_swap(__float_as_uint(x), __float_as_uint(x), false, false);
  x = fmaxf(__uint_as_float(s[0]), __uint_as_float(s[1]));
  auto t = __builtin_amdgcn_permlane32_swap(__float_as_uint(x), __float_as_uint(x), false, false);
  return fmaxf(__uint_as_float(t[0]), __uint_as_float(t[1]));
}
__device__ __forceinline__ float xrow_sum(float x) {
  auto s = __builtin_amdgcn_permlane16_swap(__float_as_uint(x), __float_as_uint(x), false, false);
  x = __uint_as_float(s[0]) + __uint_as_float(s[1]);
  auto t = __builtin_amdgcn_permlane32_swap(__float_as_uint(x), __float_as_uint(x), false, false);
  return __uint_as_float(t[0]) + __uint_as_float(t[1]);
}
#define SB() __builtin_amdgcn_sched_barrier(0)
#define WG_BARRIER() asm volatile("s_waitcnt lgkmcnt(0)\n\ts_barrier" ::: "memory")
#define LDS_WAIT() asm volatile("s_waitcnt lgkmcnt(0)" ::: "memory")
#define STAMP(i) do {} while (0)

__device__ __forceinline__ void rows8_mfma(const v4u* r, v4u a, f32x4* acc) {
#pragma unroll
  for (int d = 0; d < 4; ++d) {
    v4u lo = {pk_lo(r[0][d], r[1][d]), pk_lo(r[2][d], r[3][d]), pk_lo(r[4][d], r[5][d]), pk_lo(r[6][d], r[7][d])};
    v4u hi = {pk_hi(r[0][d], r[1][d]), pk_hi(r[2][d], r[3][d]), pk_hi(r[4][d], r[5][d]), pk_hi(r[6][d], r[7][d])};
    acc[2 * d] = mfma32(a, lo, acc[2 * d]);
    acc[2 * d + 1] = mfma32(a, hi, acc[2 * d + 1]);
  }
}
__device__ __forceinline__ void rows4_mfma(const v4u* r, v2u a, f32x4* acc) {
#pragma unroll
  for (int d = 0; d < 4; ++d) {
    v2u lo = {pk_lo(r[0][d], r[1][d]), pk_lo(r[2][d], r[3][d])};
    v2u hi = {pk_hi(r[0][d], r[1][d]), pk_hi(r[2][d], r[3][d])};
    acc[2 * d] = mfma16(a, lo, acc[2 * d]);
    acc[2 * d + 1] = mfma16(a, hi, acc[2 * d + 1]);
  }
}

template <int BR>
__device__ __forceinline__ void body(const Params& p, LAS unsigned char* L, int hg, int j, int w, int lane) {
  constexpr int NT = BR ? 3 : 2;
  constexpr int NP = BR ? 6 : 2;
  constexpr int J0 = BR ? 0 : 6;
  const int c = lane & 15, g = lane >> 4;
  STAMP(0);
  const __amdgpu_buffer_rsrc_t rws = mk(p.ws, WS_END);
  const unsigned e_raw = __builtin_amdgcn_raw_buffer_load_b32(rws, 0, WS_E + 256 * BR, AUX_SC1);

  int tile0, nt;
  if (BR) {
    const int cu0 = j < 2 ? 22 * j : 44 + 21 * (j - 2);
    const int n3 = j < 2 ? 6 : 5;
    nt = w < n3 ? 3 : 2;
    tile0 = cu0 + (w < n3 ? 3 * w : 3 * n3 + 2 * (w - n3));
  } else {
    nt = 2;
    tile0 = (SK - WIN) / 32 + (j - 6) * 16 + 2 * w;
  }
  int cb = -1, ru = -1;
  if (BR) {
    if (j < 2) { if (w == 7) cb = j; if (w == 6) ru = j == 0 ? hg : ((hg & 3) == 0 ? 32 + (hg >> 2) : -1); }
    else if (w >= 6) cb = 2 + 2 * (j - 2) + (w - 6);
  } else {
    if (w >= 3) cb = 5 * (j - 6) + (w - 3);
    if (w == 2) ru = j == 6 ? hg : ((hg & 3) == 0 ? 32 + (hg >> 2) : -1);
  }
  const size_t kvoff = ((size_t)hg * SK + (size_t)tile0 * 32) * 256;
  const __amdgpu_buffer_rsrc_t rk = mk(p.k[BR] + kvoff, nt * 8192), rv = mk(p.v[BR] + kvoff, nt * 8192);
  const __amdgpu_buffer_rsrc_t rq = mk(p.wq[BR], DM * HD * 2), rx = mk(p.x[BR], DM * 2);

  const v4u xa0 = ld128<0>(rx, g * 16, 160 * w), xa1 = ld128<0>(rx, g * 16, 160 * w + 64);
  const v2u xa2 = __builtin_amdgcn_raw_buffer_load_b64(rx, g * 8, 160 * w + 128, 0);
  v4u wq[20], kk[8 * NT], vv[8 * NT];
  {
    const int sb = 80 * w * (HD * 2) + hg * 256;
    const int vo8 = g * 8 * (HD * 2) + c * 16, vo4 = g * 4 * (HD * 2) + c * 16;
#pragma unroll
    for (int i = 0; i < 16; ++i) wq[i] = ld128<AUX_WQ>(rq, vo8, sb + ((i >> 3) * 32 + (i & 7)) * (HD * 2));
#pragma unroll
    for (int i = 0; i < 4; ++i) wq[16 + i] = ld128<AUX_WQ>(rq, vo4, sb + (64 + i) * (HD * 2));
  }
  const int kvo = (c >> 1) * 256 + (c & 1) * 64 + g * 16;
#pragma unroll
  for (int i = 0; i < 8 * NT; ++i) kk[i] = ld128<AUX_KV>(rk, kvo, (i >> 1) * 2048 + (i & 1) * 128);
  const int vvo = g * 512 + c * 16;
#define VLOAD(i) vv[i] = ld128<AUX_KV>(rv, vvo, ((i) >> 3) * 8192 + (((i) & 7) >> 1) * 2048 + ((i) & 1) * 256)
  constexpr int V0 = BR ? 4 : 12;
#pragma unroll
  for (int i = 0; i < V0; ++i) VLOAD(i);
  SB();
  STAMP(1);
  {
    f32x4 acc[8];
#pragma unroll
    for (int i = 0; i < 8; ++i) acc[i] = f32x4{0.f, 0.f, 0.f, 0.f};
    rows8_mfma(wq, xa0, acc);
    SB();
#pragma unroll
    for (int i = V0; i < V0 + 8 && i < 8 * NT; ++i) VLOAD(i);
    SB();
    rows8_mfma(wq + 8, xa1, acc);
    SB();
#pragma unroll
    for (int i = V0 + 8; i < V0 + 16 && i < 8 * NT; ++i) VLOAD(i);
    SB();
    rows4_mfma(wq + 16, xa2, acc);
    SB();
#pragma unroll
    for (int i = V0 + 16; i < 8 * NT; ++i) VLOAD(i);
    if (g == 0) {
      LAS f32x4* qp = (LAS f32x4*)(L + L_QP + w * 512 + c * 32);
      qp[0] = f32x4{acc[0][0], acc[1][0], acc[2][0], acc[3][0]};
      qp[1] = f32x4{acc[4][0], acc[5][0], acc[6][0], acc[7][0]};
    }
  }
  STAMP(2);
  WG_BARRIER();
  v4u qf0, qf1;
  {
    f32x2 s = {0.f, 0.f};
#pragma unroll
    for (int ww = 0; ww < 8; ++ww) s += *(LAS f32x2*)(L + L_QP + ww * 512 + lane * 8);
    *(LAS unsigned*)(L + L_XB + w * 256 + lane * 4) = cvtpk(s[0], s[1]);
    LDS_WAIT();
    qf0 = *(LAS v4u*)(L + L_XB + w * 256 + (c & 1) * 64 + g * 16);
    qf1 = *(LAS v4u*)(L + L_XB + w * 256 + 128 + (c & 1) * 64 + g * 16);
  }
  STAMP(3);
  float sc[8 * NT];
  const __amdgpu_buffer_rsrc_t ro = mk(p.wo[BR], cb >= 0 ? HD * DM * 2 : 0);
  v4u wo[16];
  const int wovo = ((c >> 3) * 32 + g * 8) * (DM * 2) + (c & 7) * 16;
  const int wosb = hg * 128 * (DM * 2) + cb * 128;
#pragma unroll
  for (int t = 0; t < 4 * NT; ++t) {
    f32x4 d = {0.f, 0.f, 0.f, 0.f};
    d = mfma32(kk[2 * t], qf0, d);
    d = mfma32(kk[2 * t + 1], qf1, d);
    const float s0 = d[0] + dpp<0xB1>(d[1]), s1 = d[2] + dpp<0xB1>(d[3]);
    const unsigned u = cvtpk(s0, s1);
    const unsigned u2 = cvtpk(bflo(u) * 0.08838834764831845f, bfhi(u) * 0.08838834764831845f);
    sc[2 * t] = bflo(u2);
    sc[2 * t + 1] = bfhi(u2);
  }
  SB();
#pragma unroll
  for (int i = 0; i < 16; ++i) wo[i] = ld128<AUX_WO>(ro, wovo, wosb + ((i >> 3) * 64 + (i & 7)) * (DM * 2));
  SB();
  if (BR && nt == 2) {
#pragma unroll
    for (int i = 16; i < 8 * NT; ++i) sc[i] = -INFINITY;
  }
  float mx = sc[0];
#pragma unroll
  for (int i = 1; i < 8 * NT; ++i) mx = fmaxf(mx, sc[i]);
  mx = xrow_max(mx);
  if (lane == 0) *(LAS float*)(L + L_WM + w * 4) = mx;
  STAMP(4);
  WG_BARRIER();
  float M;
  {
    const f32x4 a = *(LAS f32x4*)(L + L_WM), b = *(LAS f32x4*)(L + L_WM + 16);
    M = fmaxf(fmaxf(fmaxf(a[0], a[1]), fmaxf(a[2], a[3])), fmaxf(fmaxf(b[0], b[1]), fmaxf(b[2], b[3])));
  }
  float lsum = 0.f;
  v4u pa[NT];
#pragma unroll
  for (int T = 0; T < NT; ++T) {
#pragma unroll
    for (int t = 0; t < 4; ++t) {
      const float p0 = __builtin_amdgcn_exp2f((sc[8 * T + 2 * t] - M) * 1.4426950408889634f);
      const float p1 = __builtin_amdgcn_exp2f((sc[8 * T + 2 * t + 1] - M) * 1.4426950408889634f);
      lsum += p0; lsum += p1;
      pa[T][t] = cvtpk(p0, p1);
    }
  }
  lsum = xrow_sum(lsum);
  if (lane == 0) *(LAS float*)(L + L_WL + w * 4) = lsum;
  STAMP(5);
  {
    f32x4 acc[8];
#pragma unroll
    for (int i = 0; i < 8; ++i) acc[i] = f32x4{0.f, 0.f, 0.f, 0.f};
#pragma unroll
    for (int T = 0; T < NT; ++T) rows8_mfma(vv + 8 * T, pa[T], acc);
    if (g == 0) {
      LAS f32x4* op = (LAS f32x4*)(L + L_OP + w * 512 + c * 32);
      op[0] = f32x4{acc[0][0], acc[1][0], acc[2][0], acc[3][0]};
      op[1] = f32x4{acc[4][0], acc[5][0], acc[6][0], acc[7][0]};
    }
  }
  STAMP(6);
  const unsigned tag = __builtin_amdgcn_readfirstlane(e_raw) + 1u;
  WG_BARRIER();
  STAMP(7);
  if (w == 0) {
    f32x2 s = {0.f, 0.f};
#pragma unroll
    for (int ww = 0; ww < 8; ++ww) s += *(LAS f32x2*)(L + L_OP + ww * 512 + lane * 8);
    const v4u u = {__float_as_uint(s[0]), tag, __float_as_uint(s[1]), tag};
    __builtin_amdgcn_raw_buffer_store_b128(u, rws, lane * 16, WS_PO + (hg * 8 + j) * 1024, AUX_SC1);
  } else if (w == 1) {
    const f32x4 a = *(LAS f32x4*)(L + L_WL), b = *(LAS f32x4*)(L + L_WL + 16);
    const float l = ((a[0] + a[1]) + (a[2] + a[3])) + ((b[0] + b[1]) + (b[2] + b[3]));
    const v4u u = {__float_as_uint(M), tag, __float_as_uint(l), tag};
    if (lane == 0) __builtin_amdgcn_raw_buffer_store_b128(u, rws, 0, WS_ML + (BR * 32 + hg) * 128 + j * 16, AUX_SC1);
  } else if (cb >= 0) {
    v4u wf[16];
#pragma unroll
    for (int t = 0; t < 2; ++t)
#pragma unroll
      for (int d = 0; d < 4; ++d) {
        const v4u* r = wo + 8 * t;
        wf[8 * t + 2 * d] = v4u{pk_lo(r[0][d], r[1][d]), pk_lo(r[2][d], r[3][d]), pk_lo(r[4][d], r[5][d]), pk_lo(r[6][d], r[7][d])};
        wf[8 * t + 2 * d + 1] = v4u{pk_hi(r[0][d], r[1][d]), pk_hi(r[2][d], r[3][d]), pk_hi(r[4][d], r[5][d]), pk_hi(r[6][d], r[7][d])};
      }
    SB();
    v4u po[NP], ml;
    const int mlv = (lane < NP ? lane : 0) * 16;
    for (unsigned spins = 0;; ++spins) {
      ml = ld128<AUX_SC1>(rws, mlv, WS_ML + (BR * 32 + hg) * 128 + J0 * 16);
#pragma unroll
      for (int s = 0; s < NP; ++s) po[s] = ld128<AUX_SC1>(rws, lane * 16, WS_PO + (hg * 8 + J0 + s) * 1024);
      bool ok = ml[1] == tag && ml[3] == tag;
#pragma unroll
      for (int s = 0; s < NP; ++s) ok = ok && po[s][1] == tag && po[s][3] == tag;
      if (__all(ok) || spins > SPIN_CAP) break;
      __builtin_amdgcn_s_sleep(2);
    }
    STAMP(8);
    float ms[NP], Mg = -INFINITY;
#pragma unroll
    for (int s = 0; s < NP; ++s) { ms[s] = __uint_as_float(__builtin_amdgcn_readlane(ml[0], s)); Mg = fmaxf(Mg, ms[s]); }
    float Lg = 0.f, o0 = 0.f, o1 = 0.f;
#pragma unroll
    for (int s = 0; s < NP; ++s) {
      const float ws_ = __builtin_amdgcn_exp2f((ms[s] - Mg) * 1.4426950408889634f);
      Lg = fmaf(ws_, __uint_as_float(__builtin_amdgcn_readlane(ml[2], s)), Lg);
      o0 = fmaf(ws_, __uint_as_float(po[s][0]), o0);
      o1 = fmaf(ws_, __uint_as_float(po[s][2]), o1);
    }
    const float inv = 1.0f / Lg;
    *(LAS unsigned*)(L + L_XB + w * 256 + lane * 4) = cvtpk(o0 * inv, o1 * inv);
    LDS_WAIT();
    const v4u oa0 = *(LAS v4u*)(L + L_XB + w * 256 + (c & 1) * 64 + g * 16);
    const v4u oa1 = *(LAS v4u*)(L + L_XB + w * 256 + 128 + (c & 1) * 64 + g * 16);
    float y[8];
#pragma unroll
    for (int jj = 0; jj < 8; ++jj) {
      f32x4 d = {0.f, 0.f, 0.f, 0.f};
      d = mfma32(oa0, wf[jj], d);
      d = mfma32(oa1, wf[8 + jj], d);
      y[jj] = d[0] + dpp<0x128>(d[1]);
    }
    const float y0 = g == 0 ? y[0] : g == 1 ? y[2] : g == 2 ? y[4] : y[6];
    const float y1 = g == 0 ? y[1] : g == 1 ? y[3] : g == 2 ? y[5] : y[7];
    const v4u u = {__float_as_uint(y0), tag, __float_as_uint(y1), tag};
    if (c < 8) __builtin_amdgcn_raw_buffer_store_b128(u, rws, c * 64 + g * 16, WS_Y + ((BR * 10 + cb) * 32 + hg) * 512, AUX_SC1);
    STAMP(9);
  } else if (ru >= 0) {
    v4u yy[4];
    const int vo = (lane >> 3) * 512 + (lane & 7) * 16;
    const int so = WS_Y + (BR * 10 + (ru >> 2)) * 32 * 512 + (ru & 3) * 128;
    for (unsigned spins = 0;; ++spins) {
#pragma unroll
      for (int i = 0; i < 4; ++i) yy[i] = ld128<AUX_SC1>(rws, vo, so + i * 8 * 512);
      bool ok = true;
#pragma unroll
      for (int i = 0; i < 4; ++i) ok = ok && yy[i][1] == tag && yy[i][3] == tag;
      if (__all(ok) || spins > SPIN_CAP) break;
      __builtin_amdgcn_s_sleep(2);
    }
    STAMP(10);
    float a0 = (__uint_as_float(yy[0][0]) + __uint_as_float(yy[1][0])) + (__uint_as_float(yy[2][0]) + __uint_as_float(yy[3][0]));
    float a1 = (__uint_as_float(yy[0][2]) + __uint_as_float(yy[1][2])) + (__uint_as_float(yy[2][2]) + __uint_as_float(yy[3][2]));
    a0 += dpp<0x128>(a0); a1 += dpp<0x128>(a1);
    a0 = xrow_sum(a0); a1 = xrow_sum(a1);
    if (lane < 8) *(unsigned*)(p.out + (BR * DM + ru * 16 + lane * 2) * 2) = cvtpk(a0, a1);
    if (ru == 0 && lane == 0) __builtin_amdgcn_raw_buffer_store_b32(tag, rws, 0, WS_E + 256 * BR, AUX_SC1);
    STAMP(11);
  }
}

__global__ void __launch_bounds__(512, 2) fused_decode_kernel(Params p) {
  __shared__ __attribute__((aligned(16))) unsigned char smem[L_END];
  LAS unsigned char* L = (LAS unsigned char*)smem;
  const int b = blockIdx.x, lane = threadIdx.x & 63, w = __builtin_amdgcn_readfirstlane(threadIdx.x >> 6);
  const int hg = (b & 7) * 4 + (b >> 6), j = (b >> 3) & 7;
  if (j < 6) body<1>(p, L, hg, j, w, lane);
  else body<0>(p, L, hg, j, w, lane);
}

extern "C" void kernel_launch(void* const* d_in, const int* in_sizes, int n_in, void* d_out, int out_size, void* d_ws, size_t ws_size,
                              hipStream_t stream) {
  Params p;
  for (int br = 0; br < 2; ++br) {
    p.x[br] = (const unsigned char*)d_in[5 * br + 0];
    p.k[br] = (const unsigned char*)d_in[5 * br + 1];
    p.v[br] = (const unsigned char*)d_in[5 * br + 2];
    p.wq[br] = (const unsigned char*)d_in[5 * br + 3];
    p.wo[br] = (const unsigned char*)d_in[5 * br + 4];
  }
  p.out = (unsigned char*)d_out;
  p.ws = (unsigned char*)d_ws;
  fused_decode_kernel<<<256, 512, 0, stream>>>(p);
}
```

```cpp
#include <hip/hip_runtime.h>
#include <stdint.h>

#define AUX_KV 2
#define AUX_WQ 2
#define AUX_WO 2
#define STAMPS 0

constexpr int PK_A=2, PV_A=2, PQ_A=2, PO_A=2, PK_B=2, PV_B=2, PQ_B=2, PO_B=2;
typedef unsigned v4u __attribute__((ext_vector_type(4)));
typedef unsigned v2u __attribute__((ext_vector_type(2)));
typedef float f32x4 __attribute__((ext_vector_type(4)));
typedef float f32x2 __attribute__((ext_vector_type(2)));
typedef __bf16 bf16x8 __attribute__((ext_vector_type(8)));
typedef __bf16 bf16x2 __attribute__((ext_vector_type(2)));
typedef short s16x4 __attribute__((ext_vector_type(4)));
#define LAS __attribute__((address_space(3)))

constexpr int DM = 640, NH = 32, SK = 4096, DH = 128, WIN = 1024, HD = NH * DH;
constexpr int AUX_SC1 = 16;
constexpr unsigned RSRC_W3 = 0x00020000u;
constexpr int WS_E = 0;
constexpr int WS_ML = 4096;
constexpr int WS_PO = 16384;
constexpr int WS_Y = WS_PO + 32 * 8 * 1024;
constexpr int WS_END = WS_Y + 2 * 10 * 32 * 512;
constexpr int WS_STAMP = 1 << 20;
constexpr unsigned SPIN_CAP = 1u << 16;
constexpr int L_QP = 0;
constexpr int L_OP = 4096;
constexpr int L_XB = 8192;
constexpr int L_WM = 10240;
constexpr int L_WL = 10272;
constexpr int L_END = 10304;

struct Params {
  const unsigned char* x[2];
  const unsigned char* k[2];
  const unsigned char* v[2];
  const unsigned char* wq[2];
  const unsigned char* wo[2];
  unsigned char* out;
  unsigned char* ws;
};

__device__ __forceinline__ __amdgpu_buffer_rsrc_t mk(const unsigned char* p, unsigned bytes) {
  return __builtin_amdgcn_make_buffer_rsrc((void*)p, 0, (int)bytes, RSRC_W3);
}
template <int AUX> __device__ __forceinline__ v4u ld128(__amdgpu_buffer_rsrc_t rs, int voff, int soff) {
  return __builtin_amdgcn_raw_buffer_load_b128(rs, voff, soff, AUX);
}
__device__ __forceinline__ f32x4 mfma32(v4u a, v4u b, f32x4 c) {
  return __builtin_amdgcn_mfma_f32_16x16x32_bf16(__builtin_bit_cast(bf16x8, a), __builtin_bit_cast(bf16x8, b), c, 0, 0, 0);
}
__device__ __forceinline__ f32x4 mfma16(v2u a, v2u b, f32x4 c) {
  return __builtin_amdgcn_mfma_f32_16x16x16bf16_1k(__builtin_bit_cast(s16x4, a), __builtin_bit_cast(s16x4, b), c, 0, 0, 0);
}
__device__ __forceinline__ unsigned pk_lo(unsigned r0, unsigned r1) { return __builtin_amdgcn_perm(r1, r0, 0x05040100u); }
__device__ __forceinline__ unsigned pk_hi(unsigned r0, unsigned r1) { return __builtin_amdgcn_perm(r1, r0, 0x07060302u); }
__device__ __forceinline__ unsigned cvtpk(float lo, float hi) {
  f32x2 f = {lo, hi};
  return __builtin_bit_cast(unsigned, __builtin_convertvector(f, bf16x2));
}
__device__ __forceinline__ float bflo(unsigned u) { return __builtin_bit_cast(float, u << 16); }
__device__ __forceinline__ float bfhi(unsigned u) { return __builtin_bit_cast(float, u & 0xffff0000u); }
template <int CTRL> __device__ __forceinline__ float dpp(float x) {
  return __builtin_bit_cast(float, __builtin_amdgcn_mov_dpp(__builtin_bit_cast(int, x), CTRL, 0xf, 0xf, true));
}
__device__ __forceinline__ float xrow_max(float x) {
  auto s = __builtin_amdgcn_permlane16_swap(__float_as_uint(x), __float_as_uint(x), false, false);
  x = fmaxf(__uint_as_float(s[0]), __uint_as_float(s[1]));
  auto t = __builtin_amdgcn_permlane32_swap(__float_as_uint(x), __float_as_uint(x), false, false);
  return fmaxf(__uint_as_float(t[0]), __uint_as_float(t[1]));
}
__device__ __forceinline__ float xrow_sum(float x) {
  auto s = __builtin_amdgcn_permlane16_swap(__float_as_uint(x), __float_as_uint(x), false, false);
  x = __uint_as_float(s[0]) + __uint_as_float(s[1]);
  auto t = __builtin_amdgcn_permlane32_swap(__float_as_uint(x), __float_as_uint(x), false, false);
  return __uint_as_float(t[0]) + __uint_as_float(t[1]);
}
#define SB() __builtin_amdgcn_sched_barrier(0)
#define WG_BARRIER() asm volatile("s_waitcnt lgkmcnt(0)\n\ts_barrier" ::: "memory")
#define LDS_WAIT() asm volatile("s_waitcnt lgkmcnt(0)" ::: "memory")
#define STAMP(i) do {} while (0)

__device__ __forceinline__ void rows8_mfma(const v4u* r, v4u a, f32x4* acc) {
#pragma unroll
  for (int d = 0; d < 4; ++d) {
    v4u lo = {pk_lo(r[0][d], r[1][d]), pk_lo(r[2][d], r[3][d]), pk_lo(r[4][d], r[5][d]), pk_lo(r[6][d], r[7][d])};
    v4u hi = {pk_hi(r[0][d], r[1][d]), pk_hi(r[2][d], r[3][d]), pk_hi(r[4][d], r[5][d]), pk_hi(r[6][d], r[7][d])};
    acc[2 * d] = mfma32(a, lo, acc[2 * d]);
    acc[2 * d + 1] = mfma32(a, hi, acc[2 * d + 1]);
  }
}
__device__ __forceinline__ void rows4_mfma(const v4u* r, v2u a, f32x4* acc) {
#pragma unroll
  for (int d = 0; d < 4; ++d) {
    v2u lo = {pk_lo(r[0][d], r[1][d]), pk_lo(r[2][d], r[3][d])};
    v2u hi = {pk_hi(r[0][d], r[1][d]), pk_hi(r[2][d], r[3][d])};
    acc[2 * d] = mfma16(a, lo, acc[2 * d]);
    acc[2 * d + 1] = mfma16(a, hi, acc[2 * d + 1]);
  }
}

template <int BR>
__device__ __forceinline__ void body(const Params& p, LAS unsigned char* L, int hg, int j, int w, int lane) {
  constexpr int NT = BR ? 3 : 2;
  constexpr int NP = BR ? 6 : 2;
  constexpr int J0 = BR ? 0 : 6;
  const int c = lane & 15, g = lane >> 4;
  STAMP(0);
  const __amdgpu_buffer_rsrc_t rws = mk(p.ws, WS_END);
  const unsigned e_raw = __builtin_amdgcn_raw_buffer_load_b32(rws, 0, WS_E + 256 * BR, AUX_SC1);

  int tile0, nt;
  if (BR) {
    const int cu0 = j < 2 ? 22 * j : 44 + 21 * (j - 2);
    const int n3 = j < 2 ? 6 : 5;
    nt = w < n3 ? 3 : 2;
    tile0 = cu0 + (w < n3 ? 3 * w : 3 * n3 + 2 * (w - n3));
  } else {
    nt = 2;
    tile0 = (SK - WIN) / 32 + (j - 6) * 16 + 2 * w;
  }
  int cb = -1, ru = -1;
  if (BR) {
    if (j < 2) { if (w == 7) cb = j; if (w == 6) ru = j == 0 ? hg : ((hg & 3) == 0 ? 32 + (hg >> 2) : -1); }
    else if (w >= 6) cb = 2 + 2 * (j - 2) + (w - 6);
  } else {
    if (w >= 3) cb = 5 * (j - 6) + (w - 3);
    if (w == 2) ru = j == 6 ? hg : ((hg & 3) == 0 ? 32 + (hg >> 2) : -1);
  }
  const size_t kvoff = ((size_t)hg * SK + (size_t)tile0 * 32) * 256;
  const __amdgpu_buffer_rsrc_t rk = mk(p.k[BR] + kvoff, nt * 8192), rv = mk(p.v[BR] + kvoff, nt * 8192);
  const __amdgpu_buffer_rsrc_t rq = mk(p.wq[BR], DM * HD * 2), rx = mk(p.x[BR], DM * 2);

  const v4u xa0 = ld128<0>(rx, g * 16, 160 * w), xa1 = ld128<0>(rx, g * 16, 160 * w + 64);
  const v2u xa2 = __builtin_amdgcn_raw_buffer_load_b64(rx, g * 8, 160 * w + 128, 0);
  v4u wq[20], kk[8 * NT], vv[8 * NT];
  {
    const int sb = 80 * w * (HD * 2) + hg * 256;
    const int vo8 = g * 8 * (HD * 2) + c * 16, vo4 = g * 4 * (HD * 2) + c * 16;
#pragma unroll
    for (int i = 0; i < 16; ++i) wq[i] = ld128<BR ? PQ_B : PQ_A>(rq, vo8, sb + ((i >> 3) * 32 + (i & 7)) * (HD * 2));
#pragma unroll
    for (int i = 0; i < 4; ++i) wq[16 + i] = ld128<BR ? PQ_B : PQ_A>(rq, vo4, sb + (64 + i) * (HD * 2));
  }
  const int kvo = (c >> 1) * 256 + (c & 1) * 64 + g * 16;
#pragma unroll
  for (int i = 0; i < 8 * NT; ++i) kk[i] = ld128<BR ? PK_B : PK_A>(rk, kvo, (i >> 1) * 2048 + (i & 1) * 128);
  const int vvo = g * 512 + c * 16;
#define VLOAD(i) vv[i] = ld128<BR ? PV_B : PV_A>(rv, vvo, ((i) >> 3) * 8192 + (((i) & 7) >> 1) * 2048 + ((i) & 1) * 256)
  constexpr int V0 = BR ? 4 : 12;
#pragma unroll
  for (int i = 0; i < V0; ++i) VLOAD(i);
  SB();
  STAMP(1);
  {
    f32x4 acc[8];
#pragma unroll
    for (int i = 0; i < 8; ++i) acc[i] = f32x4{0.f, 0.f, 0.f, 0.f};
    rows8_mfma(wq, xa0, acc);
    SB();
#pragma unroll
    for (int i = V0; i < V0 + 8 && i < 8 * NT; ++i) VLOAD(i);
    SB();
    rows8_mfma(wq + 8, xa1, acc);
    SB();
#pragma unroll
    for (int i = V0 + 8; i < V0 + 16 && i < 8 * NT; ++i) VLOAD(i);
    SB();
    rows4_mfma(wq + 16, xa2, acc);
    SB();
#pragma unroll
    for (int i = V0 + 16; i < 8 * NT; ++i) VLOAD(i);
    if (g == 0) {
      LAS f32x4* qp = (LAS f32x4*)(L + L_QP + w * 512 + c * 32);
      qp[0] = f32x4{acc[0][0], acc[1][0], acc[2][0], acc[3][0]};
      qp[1] = f32x4{acc[4][0], acc[5][0], acc[6][0], acc[7][0]};
    }
  }
  STAMP(2);
  WG_BARRIER();
  v4u qf0, qf1;
  {
    f32x2 s = {0.f, 0.f};
#pragma unroll
    for (int ww = 0; ww < 8; ++ww) s += *(LAS f32x2*)(L + L_QP + ww * 512 + lane * 8);
    *(LAS unsigned*)(L + L_XB + w * 256 + lane * 4) = cvtpk(s[0], s[1]);
    LDS_WAIT();
    qf0 = *(LAS v4u*)(L + L_XB + w * 256 + (c & 1) * 64 + g * 16);
    qf1 = *(LAS v4u*)(L + L_XB + w * 256 + 128 + (c & 1) * 64 + g * 16);
  }
  STAMP(3);
  float sc[8 * NT];
  const __amdgpu_buffer_rsrc_t ro = mk(p.wo[BR], cb >= 0 ? HD * DM * 2 : 0);
  v4u wo[16];
  const int wovo = ((c >> 3) * 32 + g * 8) * (DM * 2) + (c & 7) * 16;
  const int wosb = hg * 128 * (DM * 2) + cb * 128;
#pragma unroll
  for (int t = 0; t < 4 * NT; ++t) {
    f32x4 d = {0.f, 0.f, 0.f, 0.f};
    d = mfma32(kk[2 * t], qf0, d);
    d = mfma32(kk[2 * t + 1], qf1, d);
    const float s0 = d[0] + dpp<0xB1>(d[1]), s1 = d[2] + dpp<0xB1>(d[3]);
    const unsigned u = cvtpk(s0, s1);
    const unsigned u2 = cvtpk(bflo(u) * 0.08838834764831845f, bfhi(u) * 0.08838834764831845f);
    sc[2 * t] = bflo(u2);
    sc[2 * t + 1] = bfhi(u2);
  }
  SB();
#pragma unroll
  for (int i = 0; i < 16; ++i) wo[i] = ld128<BR ? PO_B : PO_A>(ro, wovo, wosb + ((i >> 3) * 64 + (i & 7)) * (DM * 2));
  SB();
  if (BR && nt == 2) {
#pragma unroll
    for (int i = 16; i < 8 * NT; ++i) sc[i] = -INFINITY;
  }
  float mx = sc[0];
#pragma unroll
  for (int i = 1; i < 8 * NT; ++i) mx = fmaxf(mx, sc[i]);
  mx = xrow_max(mx);
  if (lane == 0) *(LAS float*)(L + L_WM + w * 4) = mx;
  STAMP(4);
  WG_BARRIER();
  float M;
  {
    const f32x4 a = *(LAS f32x4*)(L + L_WM), b = *(LAS f32x4*)(L + L_WM + 16);
    M = fmaxf(fmaxf(fmaxf(a[0], a[1]), fmaxf(a[2], a[3])), fmaxf(fmaxf(b[0], b[1]), fmaxf(b[2], b[3])));
  }
  float lsum = 0.f;
  v4u pa[NT];
#pragma unroll
  for (int T = 0; T < NT; ++T) {
#pragma unroll
    for (int t = 0; t < 4; ++t) {
      const float p0 = __builtin_amdgcn_exp2f((sc[8 * T + 2 * t] - M) * 1.4426950408889634f);
      const float p1 = __builtin_amdgcn_exp2f((sc[8 * T + 2 * t + 1] - M) * 1.4426950408889634f);
      lsum += p0; lsum += p1;
      pa[T][t] = cvtpk(p0, p1);
    }
  }
  lsum = xrow_sum(lsum);
  if (lane == 0) *(LAS float*)(L + L_WL + w * 4) = lsum;
  STAMP(5);
  {
    f32x4 acc[8];
#pragma unroll
    for (int i = 0; i < 8; ++i) acc[i] = f32x4{0.f, 0.f, 0.f, 0.f};
#pragma unroll
    for (int T = 0; T < NT; ++T) rows8_mfma(vv + 8 * T, pa[T], acc);
    if (g == 0) {
      LAS f32x4* op = (LAS f32x4*)(L + L_OP + w * 512 + c * 32);
      op[0] = f32x4{acc[0][0], acc[1][0], acc[2][0], acc[3][0]};
      op[1] = f32x4{acc[4][0], acc[5][0], acc[6][0], acc[7][0]};
    }
  }
  STAMP(6);
  const unsigned tag = __builtin_amdgcn_readfirstlane(e_raw) + 1u;
  WG_BARRIER();
  STAMP(7);
  if (w == 0) {
    f32x2 s = {0.f, 0.f};
#pragma unroll
    for (int ww = 0; ww < 8; ++ww) s += *(LAS f32x2*)(L + L_OP + ww * 512 + lane * 8);
    const v4u u = {__float_as_uint(s[0]), tag, __float_as_uint(s[1]), tag};
    __builtin_amdgcn_raw_buffer_store_b128(u, rws, lane * 16, WS_PO + (hg * 8 + j) * 1024, AUX_SC1);
  } else if (w == 1) {
    const f32x4 a = *(LAS f32x4*)(L + L_WL), b = *(LAS f32x4*)(L + L_WL + 16);
    const float l = ((a[0] + a[1]) + (a[2] + a[3])) + ((b[0] + b[1]) + (b[2] + b[3]));
    const v4u u = {__float_as_uint(M), tag, __float_as_uint(l), tag};
    if (lane == 0) __builtin_amdgcn_raw_buffer_store_b128(u, rws, 0, WS_ML + (BR * 32 + hg) * 128 + j * 16, AUX_SC1);
  } else if (cb >= 0) {
    v4u wf[16];
#pragma unroll
    for (int t = 0; t < 2; ++t)
#pragma unroll
      for (int d = 0; d < 4; ++d) {
        const v4u* r = wo + 8 * t;
        wf[8 * t + 2 * d] = v4u{pk_lo(r[0][d], r[1][d]), pk_lo(r[2][d], r[3][d]), pk_lo(r[4][d], r[5][d]), pk_lo(r[6][d], r[7][d])};
        wf[8 * t + 2 * d + 1] = v4u{pk_hi(r[0][d], r[1][d]), pk_hi(r[2][d], r[3][d]), pk_hi(r[4][d], r[5][d]), pk_hi(r[6][d], r[7][d])};
      }
    SB();
    v4u po[NP], ml;
    const int mlv = (lane < NP ? lane : 0) * 16;
    for (unsigned spins = 0;; ++spins) {
      ml = ld128<AUX_SC1>(rws, mlv, WS_ML + (BR * 32 + hg) * 128 + J0 * 16);
#pragma unroll
      for (int s = 0; s < NP; ++s) po[s] = ld128<AUX_SC1>(rws, lane * 16, WS_PO + (hg * 8 + J0 + s) * 1024);
      bool ok = ml[1] == tag && ml[3] == tag;
#pragma unroll
      for (int s = 0; s < NP; ++s) ok = ok && po[s][1] == tag && po[s][3] == tag;
      if (__all(ok) || spins > SPIN_CAP) break;
      __builtin_amdgcn_s_sleep(2);
    }
    STAMP(8);
    float ms[NP], Mg = -INFINITY;
#pragma unroll
    for (int s = 0; s < NP; ++s) { ms[s] = __uint_as_float(__builtin_amdgcn_readlane(ml[0], s)); Mg = fmaxf(Mg, ms[s]); }
    float Lg = 0.f, o0 = 0.f, o1 = 0.f;
#pragma unroll
    for (int s = 0; s < NP; ++s) {
      const float ws_ = __builtin_amdgcn_exp2f((ms[s] - Mg) * 1.4426950408889634f);
      Lg = fmaf(ws_, __uint_as_float(__builtin_amdgcn_readlane(ml[2], s)), Lg);
      o0 = fmaf(ws_, __uint_as_float(po[s][0]), o0);
      o1 = fmaf(ws_, __uint_as_float(po[s][2]), o1);
    }
    const float inv = 1.0f / Lg;
    *(LAS unsigned*)(L + L_XB + w * 256 + lane * 4) = cvtpk(o0 * inv, o1 * inv);
    LDS_WAIT();
    const v4u oa0 = *(LAS v4u*)(L + L_XB + w * 256 + (c & 1) * 64 + g * 16);
    const v4u oa1 = *(LAS v4u*)(L + L_XB + w * 256 + 128 + (c & 1) * 64 + g * 16);
    float y[8];
#pragma unroll
    for (int jj = 0; jj < 8; ++jj) {
      f32x4 d = {0.f, 0.f, 0.f, 0.f};
      d = mfma32(oa0, wf[jj], d);
      d = mfma32(oa1, wf[8 + jj], d);
      y[jj] = d[0] + dpp<0x128>(d[1]);
    }
    const float y0 = g == 0 ? y[0] : g == 1 ? y[2] : g == 2 ? y[4] : y[6];
    const float y1 = g == 0 ? y[1] : g == 1 ? y[3] : g == 2 ? y[5] : y[7];
    const v4u u = {__float_as_uint(y0), tag, __float_as_uint(y1), tag};
    if (c < 8) __builtin_amdgcn_raw_buffer_store_b128(u, rws, c * 64 + g * 16, WS_Y + ((BR * 10 + cb) * 32 + hg) * 512, AUX_SC1);
    STAMP(9);
  } else if (ru >= 0) {
    v4u yy[4];
    const int vo = (lane >> 3) * 512 + (lane & 7) * 16;
    const int so = WS_Y + (BR * 10 + (ru >> 2)) * 32 * 512 + (ru & 3) * 128;
    for (unsigned spins = 0;; ++spins) {
#pragma unroll
      for (int i = 0; i < 4; ++i) yy[i] = ld128<AUX_SC1>(rws, vo, so + i * 8 * 512);
      bool ok = true;
#pragma unroll
      for (int i = 0; i < 4; ++i) ok = ok && yy[i][1] == tag && yy[i][3] == tag;
      if (__all(ok) || spins > SPIN_CAP) break;
      __builtin_amdgcn_s_sleep(2);
    }
    STAMP(10);
    float a0 = (__uint_as_float(yy[0][0]) + __uint_as_float(yy[1][0])) + (__uint_as_float(yy[2][0]) + __uint_as_float(yy[3][0]));
    float a1 = (__uint_as_float(yy[0][2]) + __uint_as_float(yy[1][2])) + (__uint_as_float(yy[2][2]) + __uint_as_float(yy[3][2]));
    a0 += dpp<0x128>(a0); a1 += dpp<0x128>(a1);
    a0 = xrow_sum(a0); a1 = xrow_sum(a1);
    if (lane < 8) *(unsigned*)(p.out + (BR * DM + ru * 16 + lane * 2) * 2) = cvtpk(a0, a1);
    if (ru == 0 && lane == 0) __builtin_amdgcn_raw_buffer_store_b32(tag, rws, 0, WS_E + 256 * BR, AUX_SC1);
    STAMP(11);
  }
}

__global__ void __launch_bounds__(512, 2) fused_decode_kernel(Params p) {
  __shared__ __attribute__((aligned(16))) unsigned char smem[L_END];
  LAS unsigned char* L = (LAS unsigned char*)smem;
  const int b = blockIdx.x, lane = threadIdx.x & 63, w = __builtin_amdgcn_readfirstlane(threadIdx.x >> 6);
  const int hg = (b & 7) * 4 + (b >> 6), j = (b >> 3) & 7;
  if (j < 6) body<1>(p, L, hg, j, w, lane);
  else body<0>(p, L, hg, j, w, lane);
}

extern "C" void kernel_launch(void* const* d_in, const int* in_sizes, int n_in, void* d_out, int out_size, void* d_ws, size_t ws_size,
                              hipStream_t stream) {
  Params p;
  for (int br = 0; br < 2; ++br) {
    p.x[br] = (const unsigned char*)d_in[5 * br + 0];
    p.k[br] = (const unsigned char*)d_in[5 * br + 1];
    p.v[br] = (const unsigned char*)d_in[5 * br + 2];
    p.wq[br] = (const unsigned char*)d_in[5 * br + 3];
    p.wo[br] = (const unsigned char*)d_in[5 * br + 4];
  }
  p.out = (unsigned char*)d_out;
  p.ws = (unsigned char*)d_ws;
  fused_decode_kernel<<<256, 512, 0, stream>>>(p);
}
```

```cpp
#define TELE 12
#include <hip/hip_runtime.h>
#include <stdint.h>

#define AUX_KV 2
#define AUX_WQ 2
#define AUX_WO 2
#define STAMPS 0

typedef unsigned v4u __attribute__((ext_vector_type(4)));
typedef unsigned v2u __attribute__((ext_vector_type(2)));
typedef float f32x4 __attribute__((ext_vector_type(4)));
typedef float f32x2 __attribute__((ext_vector_type(2)));
typedef __bf16 bf16x8 __attribute__((ext_vector_type(8)));
typedef __bf16 bf16x2 __attribute__((ext_vector_type(2)));
typedef short s16x4 __attribute__((ext_vector_type(4)));
#define LAS __attribute__((address_space(3)))

constexpr int DM = 640, NH = 32, SK = 4096, DH = 128, WIN = 1024, HD = NH * DH;
constexpr int AUX_SC1 = 16;
constexpr unsigned RSRC_W3 = 0x00020000u;
constexpr int WS_E = 0;
constexpr int WS_ML = 4096;
constexpr int WS_PO = 16384;
constexpr int WS_Y = WS_PO + 32 * 8 * 1024;
constexpr int WS_END = WS_Y + 2 * 10 * 32 * 512;
constexpr int WS_STAMP = 1 << 20;
constexpr unsigned SPIN_CAP = 1u << 16;
constexpr int L_QP = 0;
constexpr int L_OP = 4096;
constexpr int L_XB = 8192;
constexpr int L_WM = 10240;
constexpr int L_WL = 10272;
constexpr int L_END = 10304;

struct Params {
  const unsigned char* x[2];
  const unsigned char* k[2];
  const unsigned char* v[2];
  const unsigned char* wq[2];
  const unsigned char* wo[2];
  unsigned char* out;
  unsigned char* ws;
};

__device__ __forceinline__ __amdgpu_buffer_rsrc_t mk(const unsigned char* p, unsigned bytes) {
  return __builtin_amdgcn_make_buffer_rsrc((void*)p, 0, (int)bytes, RSRC_W3);
}
template <int AUX> __device__ __forceinline__ v4u ld128(__amdgpu_buffer_rsrc_t rs, int voff, int soff) {
  return __builtin_amdgcn_raw_buffer_load_b128(rs, voff, soff, AUX);
}
__device__ __forceinline__ f32x4 mfma32(v4u a, v4u b, f32x4 c) {
  return __builtin_amdgcn_mfma_f32_16x16x32_bf16(__builtin_bit_cast(bf16x8, a), __builtin_bit_cast(bf16x8, b), c, 0, 0, 0);
}
__device__ __forceinline__ f32x4 mfma16(v2u a, v2u b, f32x4 c) {
  return __builtin_amdgcn_mfma_f32_16x16x16bf16_1k(__builtin_bit_cast(s16x4, a), __builtin_bit_cast(s16x4, b), c, 0, 0, 0);
}
__device__ __forceinline__ unsigned pk_lo(unsigned r0, unsigned r1) { return __builtin_amdgcn_perm(r1, r0, 0x05040100u); }
__device__ __forceinline__ unsigned pk_hi(unsigned r0, unsigned r1) { return __builtin_amdgcn_perm(r1, r0, 0x07060302u); }
__device__ __forceinline__ unsigned cvtpk(float lo, float hi) {
  f32x2 f = {lo, hi};
  return __builtin_bit_cast(unsigned, __builtin_convertvector(f, bf16x2));
}
__device__ __forceinline__ float bflo(unsigned u) { return __builtin_bit_cast(float, u << 16); }
__device__ __forceinline__ float bfhi(unsigned u) { return __builtin_bit_cast(float, u & 0xffff0000u); }
template <int CTRL> __device__ __forceinline__ float dpp(float x) {
  return __builtin_bit_cast(float, __builtin_amdgcn_mov_dpp(__builtin_bit_cast(int, x), CTRL, 0xf, 0xf, true));
}
__device__ __forceinline__ float xrow_max(float x) {
  auto s = __builtin_amdgcn_permlane16_swap(__float_as_uint(x), __float_as_uint(x), false, false);
  x = fmaxf(__uint_as_float(s[0]), __uint_as_float(s[1]));
  auto t = __builtin_amdgcn_permlane32_swap(__float_as_uint(x), __float_as_uint(x), false, false);
  return fmaxf(__uint_as_float(t[0]), __uint_as_float(t[1]));
}
__device__ __forceinline__ float xrow_sum(float x) {
  auto s = __builtin_amdgcn_permlane16_swap(__float_as_uint(x), __float_as_uint(x), false, false);
  x = __uint_as_float(s[0]) + __uint_as_float(s[1]);
  auto t = __builtin_amdgcn_permlane32_swap(__float_as_uint(x), __float_as_uint(x), false, false);
  return __uint_as_float(t[0]) + __uint_as_float(t[1]);
}
#define SB() __builtin_amdgcn_sched_barrier(0)
#define WG_BARRIER() asm volatile("s_waitcnt lgkmcnt(0)\n\ts_barrier" ::: "memory")
#define LDS_WAIT() asm volatile("s_waitcnt lgkmcnt(0)" ::: "memory")
#define STAMP(i) do { if ((i) == 0 || (i) == TELE) { SB(); tst[(i) == 0 ? 0 : 1] = (unsigned)__builtin_amdgcn_s_memrealtime(); SB(); } } while (0)

__device__ __forceinline__ void rows8_mfma(const v4u* r, v4u a, f32x4* acc) {
#pragma unroll
  for (int d = 0; d < 4; ++d) {
    v4u lo = {pk_lo(r[0][d], r[1][d]), pk_lo(r[2][d], r[3][d]), pk_lo(r[4][d], r[5][d]), pk_lo(r[6][d], r[7][d])};
    v4u hi = {pk_hi(r[0][d], r[1][d]), pk_hi(r[2][d], r[3][d]), pk_hi(r[4][d], r[5][d]), pk_hi(r[6][d], r[7][d])};
    acc[2 * d] = mfma32(a, lo, acc[2 * d]);
    acc[2 * d + 1] = mfma32(a, hi, acc[2 * d + 1]);
  }
}
__device__ __forceinline__ void rows4_mfma(const v4u* r, v2u a, f32x4* acc) {
#pragma unroll
  for (int d = 0; d < 4; ++d) {
    v2u lo = {pk_lo(r[0][d], r[1][d]), pk_lo(r[2][d], r[3][d])};
    v2u hi = {pk_hi(r[0][d], r[1][d]), pk_hi(r[2][d], r[3][d])};
    acc[2 * d] = mfma16(a, lo, acc[2 * d]);
    acc[2 * d + 1] = mfma16(a, hi, acc[2 * d + 1]);
  }
}

template <int BR>
__device__ __forceinline__ void body(const Params& p, LAS unsigned char* L, int hg, int j, int w, int lane) {
  constexpr int NT = BR ? 3 : 2;
  constexpr int NP = BR ? 6 : 2;
  constexpr int J0 = BR ? 0 : 6;
  const int c = lane & 15, g = lane >> 4;
  unsigned tst[2] = {0u, 0u};
  STAMP(0);
  const __amdgpu_buffer_rsrc_t rws = mk(p.ws, WS_END);
  const unsigned e_raw = __builtin_amdgcn_raw_buffer_load_b32(rws, 0, WS_E + 256 * BR, AUX_SC1);

  if (TELE == 12) { asm volatile("s_waitcnt vmcnt(0)" ::: "memory"); STAMP(12); }
  int tile0, nt;
  if (BR) {
    const int cu0 = j < 2 ? 22 * j : 44 + 21 * (j - 2);
    const int n3 = j < 2 ? 6 : 5;
    nt = w < n3 ? 3 : 2;
    tile0 = cu0 + (w < n3 ? 3 * w : 3 * n3 + 2 * (w - n3));
  } else {
    nt = 2;
    tile0 = (SK - WIN) / 32 + (j - 6) * 16 + 2 * w;
  }
  int cb = -1, ru = -1;
  if (BR) {
    if (j < 2) { if (w == 7) cb = j; if (w == 6) ru = j == 0 ? hg : ((hg & 3) == 0 ? 32 + (hg >> 2) : -1); }
    else if (w >= 6) cb = 2 + 2 * (j - 2) + (w - 6);
  } else {
    if (w >= 3) cb = 5 * (j - 6) + (w - 3);
    if (w == 2) ru = j == 6 ? hg : ((hg & 3) == 0 ? 32 + (hg >> 2) : -1);
  }
  const size_t kvoff = ((size_t)hg * SK + (size_t)tile0 * 32) * 256;
  const __amdgpu_buffer_rsrc_t rk = mk(p.k[BR] + kvoff, nt * 8192), rv = mk(p.v[BR] + kvoff, nt * 8192);
  const __amdgpu_buffer_rsrc_t rq = mk(p.wq[BR], DM * HD * 2), rx = mk(p.x[BR], DM * 2);

  const v4u xa0 = ld128<0>(rx, g * 16, 160 * w), xa1 = ld128<0>(rx, g * 16, 160 * w + 64);
  const v2u xa2 = __builtin_amdgcn_raw_buffer_load_b64(rx, g * 8, 160 * w + 128, 0);
  v4u wq[20], kk[8 * NT], vv[8 * NT];
  {
    const int sb = 80 * w * (HD * 2) + hg * 256;
    const int vo8 = g * 8 * (HD * 2) + c * 16, vo4 = g * 4 * (HD * 2) + c * 16;
#pragma unroll
    for (int i = 0; i < 16; ++i) wq[i] = ld128<AUX_WQ>(rq, vo8, sb + ((i >> 3) * 32 + (i & 7)) * (HD * 2));
#pragma unroll
    for (int i = 0; i < 4; ++i) wq[16 + i] = ld128<AUX_WQ>(rq, vo4, sb + (64 + i) * (HD * 2));
  }
  const int kvo = (c >> 1) * 256 + (c & 1) * 64 + g * 16;
#pragma unroll
  for (int i = 0; i < 8 * NT; ++i) kk[i] = ld128<AUX_KV>(rk, kvo, (i >> 1) * 2048 + (i & 1) * 128);
  const int vvo = g * 512 + c * 16;
#define VLOAD(i) vv[i] = ld128<AUX_KV>(rv, vvo, ((i) >> 3) * 8192 + (((i) & 7) >> 1) * 2048 + ((i) & 1) * 256)
  constexpr int V0 = BR ? 4 : 12;
#pragma unroll
  for (int i = 0; i < V0; ++i) VLOAD(i);
  SB();
  STAMP(1);
  {
    f32x4 acc[8];
#pragma unroll
    for (int i = 0; i < 8; ++i) acc[i] = f32x4{0.f, 0.f, 0.f, 0.f};
    rows8_mfma(wq, xa0, acc);
    SB();
#pragma unroll
    for (int i = V0; i < V0 + 8 && i < 8 * NT; ++i) VLOAD(i);
    SB();
    rows8_mfma(wq + 8, xa1, acc);
    SB();
#pragma unroll
    for (int i = V0 + 8; i < V0 + 16 && i < 8 * NT; ++i) VLOAD(i);
    SB();
    rows4_mfma(wq + 16, xa2, acc);
    SB();
#pragma unroll
    for (int i = V0 + 16; i < 8 * NT; ++i) VLOAD(i);
    if (g == 0) {
      LAS f32x4* qp = (LAS f32x4*)(L + L_QP + w * 512 + c * 32);
      qp[0] = f32x4{acc[0][0], acc[1][0], acc[2][0], acc[3][0]};
      qp[1] = f32x4{acc[4][0], acc[5][0], acc[6][0], acc[7][0]};
    }
  }
  STAMP(2);
  WG_BARRIER();
  v4u qf0, qf1;
  {
    f32x2 s = {0.f, 0.f};
#pragma unroll
    for (int ww = 0; ww < 8; ++ww) s += *(LAS f32x2*)(L + L_QP + ww * 512 + lane * 8);
    *(LAS unsigned*)(L + L_XB + w * 256 + lane * 4) = cvtpk(s[0], s[1]);
    LDS_WAIT();
    qf0 = *(LAS v4u*)(L + L_XB + w * 256 + (c & 1) * 64 + g * 16);
    qf1 = *(LAS v4u*)(L + L_XB + w * 256 + 128 + (c & 1) * 64 + g * 16);
  }
  STAMP(3);
  float sc[8 * NT];
  const __amdgpu_buffer_rsrc_t ro = mk(p.wo[BR], cb >= 0 ? HD * DM * 2 : 0);
  v4u wo[16];
  const int wovo = ((c >> 3) * 32 + g * 8) * (DM * 2) + (c & 7) * 16;
  const int wosb = hg * 128 * (DM * 2) + cb * 128;
#pragma unroll
  for (int t = 0; t < 4 * NT; ++t) {
    f32x4 d = {0.f, 0.f, 0.f, 0.f};
    d = mfma32(kk[2 * t], qf0, d);
    d = mfma32(kk[2 * t + 1], qf1, d);
    const float s0 = d[0] + dpp<0xB1>(d[1]), s1 = d[2] + dpp<0xB1>(d[3]);
    const unsigned u = cvtpk(s0, s1);
    const unsigned u2 = cvtpk(bflo(u) * 0.08838834764831845f, bfhi(u) * 0.08838834764831845f);
    sc[2 * t] = bflo(u2);
    sc[2 * t + 1] = bfhi(u2);
  }
  SB();
#pragma unroll
  for (int i = 0; i < 16; ++i) wo[i] = ld128<AUX_WO>(ro, wovo, wosb + ((i >> 3) * 64 + (i & 7)) * (DM * 2));
  SB();
  if (BR && nt == 2) {
#pragma unroll
    for (int i = 16; i < 8 * NT; ++i) sc[i] = -INFINITY;
  }
  float mx = sc[0];
#pragma unroll
  for (int i = 1; i < 8 * NT; ++i) mx = fmaxf(mx, sc[i]);
  mx = xrow_max(mx);
  if (lane == 0) *(LAS float*)(L + L_WM + w * 4) = mx;
  STAMP(4);
  WG_BARRIER();
  float M;
  {
    const f32x4 a = *(LAS f32x4*)(L + L_WM), b = *(LAS f32x4*)(L + L_WM + 16);
    M = fmaxf(fmaxf(fmaxf(a[0], a[1]), fmaxf(a[2], a[3])), fmaxf(fmaxf(b[0], b[1]), fmaxf(b[2], b[3])));
  }
  float lsum = 0.f;
  v4u pa[NT];
#pragma unroll
  for (int T = 0; T < NT; ++T) {
#pragma unroll
    for (int t = 0; t < 4; ++t) {
      const float p0 = __builtin_amdgcn_exp2f((sc[8 * T + 2 * t] - M) * 1.4426950408889634f);
      const float p1 = __builtin_amdgcn_exp2f((sc[8 * T + 2 * t + 1] - M) * 1.4426950408889634f);
      lsum += p0; lsum += p1;
      pa[T][t] = cvtpk(p0, p1);
    }
  }
  lsum = xrow_sum(lsum);
  if (lane == 0) *(LAS float*)(L + L_WL + w * 4) = lsum;
  STAMP(5);
  {
    f32x4 acc[8];
#pragma unroll
    for (int i = 0; i < 8; ++i) acc[i] = f32x4{0.f, 0.f, 0.f, 0.f};
#pragma unroll
    for (int T = 0; T < NT; ++T) rows8_mfma(vv + 8 * T, pa[T], acc);
    if (g == 0) {
      LAS f32x4* op = (LAS f32x4*)(L + L_OP + w * 512 + c * 32);
      op[0] = f32x4{acc[0][0], acc[1][0], acc[2][0], acc[3][0]};
      op[1] = f32x4{acc[4][0], acc[5][0], acc[6][0], acc[7][0]};
    }
  }
  STAMP(6);
  const unsigned tag = __builtin_amdgcn_readfirstlane(e_raw) + 1u;
  WG_BARRIER();
  STAMP(7);
  if (w == 0) {
    f32x2 s = {0.f, 0.f};
#pragma unroll
    for (int ww = 0; ww < 8; ++ww) s += *(LAS f32x2*)(L + L_OP + ww * 512 + lane * 8);
    const v4u u = {__float_as_uint(s[0]), tag, __float_as_uint(s[1]), tag};
    __builtin_amdgcn_raw_buffer_store_b128(u, rws, lane * 16, WS_PO + (hg * 8 + j) * 1024, AUX_SC1);
  } else if (w == 1) {
    const f32x4 a = *(LAS f32x4*)(L + L_WL), b = *(LAS f32x4*)(L + L_WL + 16);
    const float l = ((a[0] + a[1]) + (a[2] + a[3])) + ((b[0] + b[1]) + (b[2] + b[3]));
    const v4u u = {__float_as_uint(M), tag, __float_as_uint(l), tag};
    if (lane == 0) __builtin_amdgcn_raw_buffer_store_b128(u, rws, 0, WS_ML + (BR * 32 + hg) * 128 + j * 16, AUX_SC1);
  } else if (cb >= 0) {
    v4u wf[16];
#pragma unroll
    for (int t = 0; t < 2; ++t)
#pragma unroll
      for (int d = 0; d < 4; ++d) {
        const v4u* r = wo + 8 * t;
        wf[8 * t + 2 * d] = v4u{pk_lo(r[0][d], r[1][d]), pk_lo(r[2][d], r[3][d]), pk_lo(r[4][d], r[5][d]), pk_lo(r[6][d], r[7][d])};
        wf[8 * t + 2 * d + 1] = v4u{pk_hi(r[0][d], r[1][d]), pk_hi(r[2][d], r[3][d]), pk_hi(r[4][d], r[5][d]), pk_hi(r[6][d], r[7][d])};
      }
    SB();
    v4u po[NP], ml;
    const int mlv = (lane < NP ? lane : 0) * 16;
    for (unsigned spins = 0;; ++spins) {
      ml = ld128<AUX_SC1>(rws, mlv, WS_ML + (BR * 32 + hg) * 128 + J0 * 16);
#pragma unroll
      for (int s = 0; s < NP; ++s) po[s] = ld128<AUX_SC1>(rws, lane * 16, WS_PO + (hg * 8 + J0 + s) * 1024);
      bool ok = ml[1] == tag && ml[3] == tag;
#pragma unroll
      for (int s = 0; s < NP; ++s) ok = ok && po[s][1] == tag && po[s][3] == tag;
      if (__all(ok) || spins > SPIN_CAP) break;
      __builtin_amdgcn_s_sleep(2);
    }
    STAMP(8);
    float ms[NP], Mg = -INFINITY;
#pragma unroll
    for (int s = 0; s < NP; ++s) { ms[s] = __uint_as_float(__builtin_amdgcn_readlane(ml[0], s)); Mg = fmaxf(Mg, ms[s]); }
    float Lg = 0.f, o0 = 0.f, o1 = 0.f;
#pragma unroll
    for (int s = 0; s < NP; ++s) {
      const float ws_ = __builtin_amdgcn_exp2f((ms[s] - Mg) * 1.4426950408889634f);
      Lg = fmaf(ws_, __uint_as_float(__builtin_amdgcn_readlane(ml[2], s)), Lg);
      o0 = fmaf(ws_, __uint_as_float(po[s][0]), o0);
      o1 = fmaf(ws_, __uint_as_float(po[s][2]), o1);
    }
    const float inv = 1.0f / Lg;
    *(LAS unsigned*)(L + L_XB + w * 256 + lane * 4) = cvtpk(o0 * inv, o1 * inv);
    LDS_WAIT();
    const v4u oa0 = *(LAS v4u*)(L + L_XB + w * 256 + (c & 1) * 64 + g * 16);
    const v4u oa1 = *(LAS v4u*)(L + L_XB + w * 256 + 128 + (c & 1) * 64 + g * 16);
    float y[8];
#pragma unroll
    for (int jj = 0; jj < 8; ++jj) {
      f32x4 d = {0.f, 0.f, 0.f, 0.f};
      d = mfma32(oa0, wf[jj], d);
      d = mfma32(oa1, wf[8 + jj], d);
      y[jj] = d[0] + dpp<0x128>(d[1]);
    }
    const float y0 = g == 0 ? y[0] : g == 1 ? y[2] : g == 2 ? y[4] : y[6];
    const float y1 = g == 0 ? y[1] : g == 1 ? y[3] : g == 2 ? y[5] : y[7];
    const v4u u = {__float_as_uint(y0), tag, __float_as_uint(y1), tag};
    if (c < 8) __builtin_amdgcn_raw_buffer_store_b128(u, rws, c * 64 + g * 16, WS_Y + ((BR * 10 + cb) * 32 + hg) * 512, AUX_SC1);
    STAMP(9);
  } else if (ru >= 0) {
    v4u yy[4];
    const int vo = (lane >> 3) * 512 + (lane & 7) * 16;
    const int so = WS_Y + (BR * 10 + (ru >> 2)) * 32 * 512 + (ru & 3) * 128;
    for (unsigned spins = 0;; ++spins) {
#pragma unroll
      for (int i = 0; i < 4; ++i) yy[i] = ld128<AUX_SC1>(rws, vo, so + i * 8 * 512);
      bool ok = true;
#pragma unroll
      for (int i = 0; i < 4; ++i) ok = ok && yy[i][1] == tag && yy[i][3] == tag;
      if (__all(ok) || spins > SPIN_CAP) break;
      __builtin_amdgcn_s_sleep(2);
    }
    STAMP(10);
    float a0 = (__uint_as_float(yy[0][0]) + __uint_as_float(yy[1][0])) + (__uint_as_float(yy[2][0]) + __uint_as_float(yy[3][0]));
    float a1 = (__uint_as_float(yy[0][2]) + __uint_as_float(yy[1][2])) + (__uint_as_float(yy[2][2]) + __uint_as_float(yy[3][2]));
    a0 += dpp<0x128>(a0); a1 += dpp<0x128>(a1);
    a0 = xrow_sum(a0); a1 = xrow_sum(a1);
    if (lane < 8) *(unsigned*)(p.out + (BR * DM + ru * 16 + lane * 2) * 2) = cvtpk(a0, a1);
    if (ru == 0 && lane == 0) __builtin_amdgcn_raw_buffer_store_b32(tag, rws, 0, WS_E + 256 * BR, AUX_SC1);
    STAMP(11);
    if (BR == 1 && ru == 0) {
      const unsigned t2 = (unsigned)__builtin_amdgcn_s_memrealtime();
      const unsigned d = (tst[1] - tst[0]) * 10u;
      while ((unsigned)__builtin_amdgcn_s_memrealtime() - t2 < d) __builtin_amdgcn_s_sleep(4);
    }
  }
}

__global__ void __launch_bounds__(512, 2) fused_decode_kernel(Params p) {
  __shared__ __attribute__((aligned(16))) unsigned char smem[L_END];
  LAS unsigned char* L = (LAS unsigned char*)smem;
  const int b = blockIdx.x, lane = threadIdx.x & 63, w = __builtin_amdgcn_readfirstlane(threadIdx.x >> 6);
  const int hg = (b & 7) * 4 + (b >> 6), j = (b >> 3) & 7;
  if (j < 6) body<1>(p, L, hg, j, w, lane);
  else body<0>(p, L, hg, j, w, lane);
}

extern "C" void kernel_launch(void* const* d_in, const int* in_sizes, int n_in, void* d_out, int out_size, void* d_ws, size_t ws_size,
                              hipStream_t stream) {
  Params p;
  for (int br = 0; br < 2; ++br) {
    p.x[br] = (const unsigned char*)d_in[5 * br + 0];
    p.k[br] = (const unsigned char*)d_in[5 * br + 1];
    p.v[br] = (const unsigned char*)d_in[5 * br + 2];
    p.wq[br] = (const unsigned char*)d_in[5 * br + 3];
    p.wo[br] = (const unsigned char*)d_in[5 * br + 4];
  }
  p.out = (unsigned char*)d_out;
  p.ws = (unsigned char*)d_ws;
  fused_decode_kernel<<<256, 512, 0, stream>>>(p);
}
```

```cpp
#include <hip/hip_runtime.h>
#include <stdint.h>

#define AUX_KV 2
#define AUX_WQ 2
#define AUX_WO 2
#define STAMPS 0

typedef unsigned v4u __attribute__((ext_vector_type(4)));
typedef unsigned v2u __attribute__((ext_vector_type(2)));
typedef float f32x4 __attribute__((ext_vector_type(4)));
typedef float f32x2 __attribute__((ext_vector_type(2)));
typedef __bf16 bf16x8 __attribute__((ext_vector_type(8)));
typedef __bf16 bf16x2 __attribute__((ext_vector_type(2)));
typedef short s16x4 __attribute__((ext_vector_type(4)));
#define LAS __attribute__((address_space(3)))

constexpr int DM = 640, NH = 32, SK = 4096, DH = 128, WIN = 1024, HD = NH * DH;
constexpr int AUX_SC1 = 16;
constexpr unsigned RSRC_W3 = 0x00020000u;
constexpr int WS_E = 0;
constexpr int WS_ML = 4096;
constexpr int WS_PO = 16384;
constexpr int WS_Y = WS_PO + 32 * 8 * 1024;
constexpr int WS_END = WS_Y + 2 * 10 * 32 * 512;
constexpr int WS_STAMP = 1 << 20;
constexpr unsigned SPIN_CAP = 1u << 16;
constexpr int L_QP = 0;
constexpr int L_OP = 4096;
constexpr int L_XB = 8192;
constexpr int L_WM = 10240;
constexpr int L_WL = 10272;
constexpr int L_END = 10304;

struct Params {
  const unsigned char* x[2];
  const unsigned char* k[2];
  const unsigned char* v[2];
  const unsigned char* wq[2];
  const unsigned char* wo[2];
  unsigned char* out;
  unsigned char* ws;
};

__device__ __forceinline__ __amdgpu_buffer_rsrc_t mk(const unsigned char* p, unsigned bytes) {
  return __builtin_amdgcn_make_buffer_rsrc((void*)p, 0, (int)bytes, RSRC_W3);
}
template <int AUX> __device__ __forceinline__ v4u ld128(__amdgpu_buffer_rsrc_t rs, int voff, int soff) {
  return __builtin_amdgcn_raw_buffer_load_b128(rs, voff, soff, AUX);
}
__device__ __forceinline__ f32x4 mfma32(v4u a, v4u b, f32x4 c) {
  return __builtin_amdgcn_mfma_f32_16x16x32_bf16(__builtin_bit_cast(bf16x8, a), __builtin_bit_cast(bf16x8, b), c, 0, 0, 0);
}
__device__ __forceinline__ f32x4 mfma16(v2u a, v2u b, f32x4 c) {
  return __builtin_amdgcn_mfma_f32_16x16x16bf16_1k(__builtin_bit_cast(s16x4, a), __builtin_bit_cast(s16x4, b), c, 0, 0, 0);
}
__device__ __forceinline__ unsigned pk_lo(unsigned r0, unsigned r1) { return __builtin_amdgcn_perm(r1, r0, 0x05040100u); }
__device__ __forceinline__ unsigned pk_hi(unsigned r0, unsigned r1) { return __builtin_amdgcn_perm(r1, r0, 0x07060302u); }
__device__ __forceinline__ unsigned cvtpk(float lo, float hi) {
  f32x2 f = {lo, hi};
  return __builtin_bit_cast(unsigned, __builtin_convertvector(f, bf16x2));
}
__device__ __forceinline__ float bflo(unsigned u) { return __builtin_bit_cast(float, u << 16); }
__device__ __forceinline__ float bfhi(unsigned u) { return __builtin_bit_cast(float, u & 0xffff0000u); }
template <int CTRL> __device__ __forceinline__ float dpp(float x) {
  return __builtin_bit_cast(float, __builtin_amdgcn_mov_dpp(__builtin_bit_cast(int, x), CTRL, 0xf, 0xf, true));
}
__device__ __forceinline__ float xrow_max(float x) {
  auto s = __builtin_amdgcn_permlane16_swap(__float_as_uint(x), __float_as_uint(x), false, false);
  x = fmaxf(__uint_as_float(s[0]), __uint_as_float(s[1]));
  auto t = __builtin_amdgcn_permlane32_swap(__float_as_uint(x), __float_as_uint(x), false, false);
  return fmaxf(__uint_as_float(t[0]), __uint_as_float(t[1]));
}
__device__ __forceinline__ float xrow_sum(float x) {
  auto s = __builtin_amdgcn_permlane16_swap(__float_as_uint(x), __float_as_uint(x), false, false);
  x = __uint_as_float(s[0]) + __uint_as_float(s[1]);
  auto t = __builtin_amdgcn_permlane32_swap(__float_as_uint(x), __float_as_uint(x), false, false);
  return __uint_as_float(t[0]) + __uint_as_float(t[1]);
}
#define SB() __builtin_amdgcn_sched_barrier(0)
#define WG_BARRIER() asm volatile("s_waitcnt lgkmcnt(0)\n\ts_barrier" ::: "memory")
#define LDS_WAIT() asm volatile("s_waitcnt lgkmcnt(0)" ::: "memory")
#define STAMP(i) do {} while (0)

__device__ __forceinline__ void rows8_mfma(const v4u* r, v4u a, f32x4* acc) {
#pragma unroll
  for (int d = 0; d < 4; ++d) {
    v4u lo = {pk_lo(r[0][d], r[1][d]), pk_lo(r[2][d], r[3][d]), pk_lo(r[4][d], r[5][d]), pk_lo(r[6][d], r[7][d])};
    v4u hi = {pk_hi(r[0][d], r[1][d]), pk_hi(r[2][d], r[3][d]), pk_hi(r[4][d], r[5][d]), pk_hi(r[6][d], r[7][d])};
    acc[2 * d] = mfma32(a, lo, acc[2 * d]);
    acc[2 * d + 1] = mfma32(a, hi, acc[2 * d + 1]);
  }
}
__device__ __forceinline__ void rows4_mfma(const v4u* r, v2u a, f32x4* acc) {
#pragma unroll
  for (int d = 0; d < 4; ++d) {
    v2u lo = {pk_lo(r[0][d], r[1][d]), pk_lo(r[2][d], r[3][d])};
    v2u hi = {pk_hi(r[0][d], r[1][d]), pk_hi(r[2][d], r[3][d])};
    acc[2 * d] = mfma16(a, lo, acc[2 * d]);
    acc[2 * d + 1] = mfma16(a, hi, acc[2 * d + 1]);
  }
}

template <int BR>
__device__ __forceinline__ void body(const Params& p, LAS unsigned char* L, int hg, int j, int w, int lane) {
  constexpr int NT = BR ? 3 : 2;
  constexpr int NP = BR ? 6 : 2;
  constexpr int J0 = BR ? 0 : 6;
  const int c = lane & 15, g = lane >> 4;
  STAMP(0);
  const __amdgpu_buffer_rsrc_t rws = mk(p.ws, WS_END);
  const unsigned e_raw = __builtin_amdgcn_raw_buffer_load_b32(rws, 0, WS_E + 256 * BR, AUX_SC1);

  int tile0, nt;
  if (BR) {
    const int cu0 = j < 2 ? 22 * j : 44 + 21 * (j - 2);
    const int n3 = j < 2 ? 6 : 5;
    nt = w < n3 ? 3 : 2;
    tile0 = cu0 + (w < n3 ? 3 * w : 3 * n3 + 2 * (w - n3));
  } else {
    nt = 2;
    tile0 = (SK - WIN) / 32 + (j - 6) * 16 + 2 * w;
  }
  int cb = -1, ru = -1;
  if (BR) {
    if (j < 2) { if (w == 7) cb = j; if (w == 6) ru = j == 0 ? hg : ((hg & 3) == 0 ? 32 + (hg >> 2) : -1); }
    else if (w >= 6) cb = 2 + 2 * (j - 2) + (w - 6);
  } else {
    if (w >= 3) cb = 5 * (j - 6) + (w - 3);
    if (w == 2) ru = j == 6 ? hg : ((hg & 3) == 0 ? 32 + (hg >> 2) : -1);
  }
  const size_t kvoff = ((size_t)hg * SK + (size_t)tile0 * 32) * 256;
  const __amdgpu_buffer_rsrc_t rk = mk(p.k[BR] + kvoff, nt * 8192), rv = mk(p.v[BR] + kvoff, nt * 8192);
  const __amdgpu_buffer_rsrc_t rq = mk(p.wq[BR], DM * HD * 2), rx = mk(p.x[BR], DM * 2);

  const v4u xa0 = ld128<0>(rx, g * 16, 160 * w), xa1 = ld128<0>(rx, g * 16, 160 * w + 64);
  const v2u xa2 = __builtin_amdgcn_raw_buffer_load_b64(rx, g * 8, 160 * w + 128, 0);
  v4u wq[20], kk[8 * NT], vv[8 * NT];
  {
    const int sb = 80 * w * (HD * 2) + hg * 256;
    const int vo8 = g * 8 * (HD * 2) + c * 16, vo4 = g * 4 * (HD * 2) + c * 16;
#pragma unroll
    for (int i = 0; i < 16; ++i) wq[i] = ld128<AUX_WQ>(rq, vo8, sb + ((i >> 3) * 32 + (i & 7)) * (HD * 2));
#pragma unroll
    for (int i = 0; i < 4; ++i) wq[16 + i] = ld128<AUX_WQ>(rq, vo4, sb + (64 + i) * (HD * 2));
  }
  const int kvo = (c >> 1) * 256 + (c & 1) * 64 + g * 16;
#pragma unroll
  for (int i = 0; i < 8 * NT; ++i) kk[i] = ld128<AUX_KV>(rk, kvo, (i >> 1) * 2048 + (i & 1) * 128);
  const int vvo = g * 512 + c * 16;
#define VLOAD(i) vv[i] = ld128<AUX_KV>(rv, vvo, ((i) >> 3) * 8192 + (((i) & 7) >> 1) * 2048 + ((i) & 1) * 256)
  constexpr int V0 = BR ? 4 : 12;
#pragma unroll
  for (int i = 0; i < V0; ++i) VLOAD(i);
  SB();
  STAMP(1);
  {
    f32x4 acc[8];
#pragma unroll
    for (int i = 0; i < 8; ++i) acc[i] = f32x4{0.f, 0.f, 0.f, 0.f};
    rows8_mfma(wq, xa0, acc);
    SB();
#pragma unroll
    for (int i = V0; i < V0 + 8 && i < 8 * NT; ++i) VLOAD(i);
    SB();
    rows8_mfma(wq + 8, xa1, acc);
    SB();
#pragma unroll
    for (int i = V0 + 8; i < V0 + 16 && i < 8 * NT; ++i) VLOAD(i);
    SB();
    rows4_mfma(wq + 16, xa2, acc);
    SB();
#pragma unroll
    for (int i = V0 + 16; i < 8 * NT; ++i) VLOAD(i);
    if (g == 0) {
      LAS f32x4* qp = (LAS f32x4*)(L + L_QP + w * 512 + c * 32);
      qp[0] = f32x4{acc[0][0], acc[1][0], acc[2][0], acc[3][0]};
      qp[1] = f32x4{acc[4][0], acc[5][0], acc[6][0], acc[7][0]};
    }
  }
  STAMP(2);
  WG_BARRIER();
  v4u qf0, qf1;
  {
    f32x2 s = {0.f, 0.f};
#pragma unroll
    for (int ww = 0; ww < 8; ++ww) s += *(LAS f32x2*)(L + L_QP + ww * 512 + lane * 8);
    *(LAS unsigned*)(L + L_XB + w * 256 + lane * 4) = cvtpk(s[0], s[1]);
    LDS_WAIT();
    qf0 = *(LAS v4u*)(L + L_XB + w * 256 + (c & 1) * 64 + g * 16);
    qf1 = *(LAS v4u*)(L + L_XB + w * 256 + 128 + (c & 1) * 64 + g * 16);
  }
  STAMP(3);
  float sc[8 * NT];
  const __amdgpu_buffer_rsrc_t ro = mk(p.wo[BR], cb >= 0 ? HD * DM * 2 : 0);
  v4u wo[16];
  const int wovo = ((c >> 3) * 32 + g * 8) * (DM * 2) + (c & 7) * 16;
  const int wosb = hg * 128 * (DM * 2) + cb * 128;
#pragma unroll
  for (int t = 0; t < 4 * NT; ++t) {
    f32x4 d = {0.f, 0.f, 0.f, 0.f};
    d = mfma32(kk[2 * t], qf0, d);
    d = mfma32(kk[2 * t + 1], qf1, d);
    const float s0 = d[0] + dpp<0xB1>(d[1]), s1 = d[2] + dpp<0xB1>(d[3]);
    const unsigned u = cvtpk(s0, s1);
    const unsigned u2 = cvtpk(bflo(u) * 0.08838834764831845f, bfhi(u) * 0.08838834764831845f);
    sc[2 * t] = bflo(u2);
    sc[2 * t + 1] = bfhi(u2);
  }
  SB();
#pragma unroll
  for (int i = 0; i < 16; ++i) wo[i] = ld128<AUX_WO>(ro, wovo, wosb + ((i >> 3) * 64 + (i & 7)) * (DM * 2));
  SB();
  if (BR && nt == 2) {
#pragma unroll
    for (int i = 16; i < 8 * NT; ++i) sc[i] = -INFINITY;
  }
  float mx = sc[0];
#pragma unroll
  for (int i = 1; i < 8 * NT; ++i) mx = fmaxf(mx, sc[i]);
  mx = xrow_max(mx);
  if (lane == 0) *(LAS float*)(L + L_WM + w * 4) = mx;
  STAMP(4);
  WG_BARRIER();
  float M;
  {
    const f32x4 a = *(LAS f32x4*)(L + L_WM), b = *(LAS f32x4*)(L + L_WM + 16);
    M = fmaxf(fmaxf(fmaxf(a[0], a[1]), fmaxf(a[2], a[3])), fmaxf(fmaxf(b[0], b[1]), fmaxf(b[2], b[3])));
  }
  float lsum = 0.f;
  v4u pa[NT];
#pragma unroll
  for (int T = 0; T < NT; ++T) {
#pragma unroll
    for (int t = 0; t < 4; ++t) {
      const float p0 = __builtin_amdgcn_exp2f((sc[8 * T + 2 * t] - M) * 1.4426950408889634f);
      const float p1 = __builtin_amdgcn_exp2f((sc[8 * T + 2 * t + 1] - M) * 1.4426950408889634f);
      lsum += p0; lsum += p1;
      pa[T][t] = cvtpk(p0, p1);
    }
  }
  lsum = xrow_sum(lsum);
  if (lane == 0) *(LAS float*)(L + L_WL + w * 4) = lsum;
  STAMP(5);
  {
    f32x4 acc[8];
#pragma unroll
    for (int i = 0; i < 8; ++i) acc[i] = f32x4{0.f, 0.f, 0.f, 0.f};
#pragma unroll
    for (int T = 0; T < NT; ++T) rows8_mfma(vv + 8 * T, pa[T], acc);
    if (g == 0) {
      LAS f32x4* op = (LAS f32x4*)(L + L_OP + w * 512 + c * 32);
      op[0] = f32x4{acc[0][0], acc[1][0], acc[2][0], acc[3][0]};
      op[1] = f32x4{acc[4][0], acc[5][0], acc[6][0], acc[7][0]};
    }
  }
  STAMP(6);
  const unsigned tag = __builtin_amdgcn_readfirstlane(e_raw) + 1u;
  WG_BARRIER();
  STAMP(7);
  if (w == 0) {
    f32x2 s = {0.f, 0.f};
#pragma unroll
    for (int ww = 0; ww < 8; ++ww) s += *(LAS f32x2*)(L + L_OP + ww * 512 + lane * 8);
    const v4u u = {__float_as_uint(s[0]), tag, __float_as_uint(s[1]), tag};
    __builtin_amdgcn_raw_buffer_store_b128(u, rws, lane * 16, WS_PO + (hg * 8 + j) * 1024, AUX_SC1);
  } else if (w == 1) {
    const f32x4 a = *(LAS f32x4*)(L + L_WL), b = *(LAS f32x4*)(L + L_WL + 16);
    const float l = ((a[0] + a[1]) + (a[2] + a[3])) + ((b[0] + b[1]) + (b[2] + b[3]));
    const v4u u = {__float_as_uint(M), tag, __float_as_uint(l), tag};
    if (lane == 0) __builtin_amdgcn_raw_buffer_store_b128(u, rws, 0, WS_ML + (BR * 32 + hg) * 128 + j * 16, AUX_SC1);
  } else if (cb >= 0) {
    v4u wf[16];
#pragma unroll
    for (int t = 0; t < 2; ++t)
#pragma unroll
      for (int d = 0; d < 4; ++d) {
        const v4u* r = wo + 8 * t;
        wf[8 * t + 2 * d] = v4u{pk_lo(r[0][d], r[1][d]), pk_lo(r[2][d], r[3][d]), pk_lo(r[4][d], r[5][d]), pk_lo(r[6][d], r[7][d])};
        wf[8 * t + 2 * d + 1] = v4u{pk_hi(r[0][d], r[1][d]), pk_hi(r[2][d], r[3][d]), pk_hi(r[4][d], r[5][d]), pk_hi(r[6][d], r[7][d])};
      }
    SB();
    v4u po[NP], ml;
    const int mlv = (lane < NP ? lane : 0) * 16;
    for (unsigned spins = 0;; ++spins) {
      ml = ld128<AUX_SC1>(rws, mlv, WS_ML + (BR * 32 + hg) * 128 + J0 * 16);
#pragma unroll
      for (int s = 0; s < NP; ++s) po[s] = ld128<AUX_SC1>(rws, lane * 16, WS_PO + (hg * 8 + J0 + s) * 1024);
      bool ok = ml[1] == tag && ml[3] == tag;
#pragma unroll
      for (int s = 0; s < NP; ++s) ok = ok && po[s][1] == tag && po[s][3] == tag;
      if (__all(ok) || spins > SPIN_CAP) break;
      __builtin_amdgcn_s_sleep(2);
    }
    STAMP(8);
    float ms[NP], Mg = -INFINITY;
#pragma unroll
    for (int s = 0; s < NP; ++s) { ms[s] = __uint_as_float(__builtin_amdgcn_readlane(ml[0], s)); Mg = fmaxf(Mg, ms[s]); }
    float Lg = 0.f, o0 = 0.f, o1 = 0.f;
#pragma unroll
    for (int s = 0; s < NP; ++s) {
      const float ws_ = __builtin_amdgcn_exp2f((ms[s] - Mg) * 1.4426950408889634f);
      Lg = fmaf(ws_, __uint_as_float(__builtin_amdgcn_readlane(ml[2], s)), Lg);
      o0 = fmaf(ws_, __uint_as_float(po[s][0]), o0);
      o1 = fmaf(ws_, __uint_as_float(po[s][2]), o1);
    }
    const float inv = 1.0f / Lg;
    *(LAS unsigned*)(L + L_XB + w * 256 + lane * 4) = cvtpk(o0 * inv, o1 * inv);
    LDS_WAIT();
    const v4u oa0 = *(LAS v4u*)(L + L_XB + w * 256 + (c & 1) * 64 + g * 16);
    const v4u oa1 = *(LAS v4u*)(L + L_XB + w * 256 + 128 + (c & 1) * 64 + g * 16);
    float y[8];
#pragma unroll
    for (int jj = 0; jj < 8; ++jj) {
      f32x4 d = {0.f, 0.f, 0.f, 0.f};
      d = mfma32(oa0, wf[jj], d);
      d = mfma32(oa1, wf[8 + jj], d);
      y[jj] = d[0] + dpp<0x128>(d[1]);
    }
    const float y0 = g == 0 ? y[0] : g == 1 ? y[2] : g == 2 ? y[4] : y[6];
    const float y1 = g == 0 ? y[1] : g == 1 ? y[3] : g == 2 ? y[5] : y[7];
    const v4u u = {__float_as_uint(y0), tag, __float_as_uint(y1), tag};
    if (c < 8) __builtin_amdgcn_raw_buffer_store_b128(u, rws, c * 64 + g * 16, WS_Y + ((BR * 10 + cb) * 32 + hg) * 512, AUX_SC1);
    STAMP(9);
  } else if (ru >= 0) {
    v4u yy[4];
    const int vo = (lane >> 3) * 512 + (lane & 7) * 16;
    const int so = WS_Y + (BR * 10 + (ru >> 2)) * 32 * 512 + (ru & 3) * 128;
    for (unsigned spins = 0;; ++spins) {
#pragma unroll
      for (int i = 0; i < 4; ++i) yy[i] = ld128<AUX_SC1>(rws, vo, so + i * 8 * 512);
      bool ok = true;
#pragma unroll
      for (int i = 0; i < 4; ++i) ok = ok && yy[i][1] == tag && yy[i][3] == tag;
      if (__all(ok) || spins > SPIN_CAP) break;
      __builtin_amdgcn_s_sleep(2);
    }
    STAMP(10);
    float a0 = (__uint_as_float(yy[0][0]) + __uint_as_float(yy[1][0])) + (__uint_as_float(yy[2][0]) + __uint_as_float(yy[3][0]));
    float a1 = (__uint_as_float(yy[0][2]) + __uint_as_float(yy[1][2])) + (__uint_as_float(yy[2][2]) + __uint_as_float(yy[3][2]));
    a0 += dpp<0x128>(a0); a1 += dpp<0x128>(a1);
    a0 = xrow_sum(a0); a1 = xrow_sum(a1);
    if (lane < 8) *(unsigned*)(p.out + (BR * DM + ru * 16 + lane * 2) * 2) = cvtpk(a0, a1);
    if (ru == 0 && lane == 0) __builtin_amdgcn_raw_buffer_store_b32(tag, rws, 0, WS_E + 256 * BR, AUX_SC1);
    STAMP(11);
  }
}

__global__ void __launch_bounds__(512, 2) fused_decode_kernel(Params p) {
  __shared__ __attribute__((aligned(16))) unsigned char smem[L_END];
  LAS unsigned char* L = (LAS unsigned char*)smem;
  const int b = blockIdx.x, lane = threadIdx.x & 63, w = __builtin_amdgcn_readfirstlane(threadIdx.x >> 6);
  const int hg = (b & 7) * 4 + (b >> 6), j = (b >> 3) & 7;
  if (b < 8 && w == 7) { asm volatile("buffer_wbl2 sc1\n\ts_waitcnt vmcnt(0)" ::: "memory"); }
  if (j < 6) body<1>(p, L, hg, j, w, lane);
  else body<0>(p, L, hg, j, w, lane);
}

extern "C" void kernel_launch(void* const* d_in, const int* in_sizes, int n_in, void* d_out, int out_size, void* d_ws, size_t ws_size,
                              hipStream_t stream) {
  Params p;
  for (int br = 0; br < 2; ++br) {
    p.x[br] = (const unsigned char*)d_in[5 * br + 0];
    p.k[br] = (const unsigned char*)d_in[5 * br + 1];
    p.v[br] = (const unsigned char*)d_in[5 * br + 2];
    p.wq[br] = (const unsigned char*)d_in[5 * br + 3];
    p.wo[br] = (const unsigned char*)d_in[5 * br + 4];
  }
  p.out = (unsigned char*)d_out;
  p.ws = (unsigned char*)d_ws;
  fused_decode_kernel<<<256, 512, 0, stream>>>(p);
}
```

```cpp
#define TELE 21
#include <hip/hip_runtime.h>
#include <stdint.h>

#define AUX_KV 2
#define AUX_WQ 2
#define AUX_WO 2
#define STAMPS 0

typedef unsigned v4u __attribute__((ext_vector_type(4)));
typedef unsigned v2u __attribute__((ext_vector_type(2)));
typedef float f32x4 __attribute__((ext_vector_type(4)));
typedef float f32x2 __attribute__((ext_vector_type(2)));
typedef __bf16 bf16x8 __attribute__((ext_vector_type(8)));
typedef __bf16 bf16x2 __attribute__((ext_vector_type(2)));
typedef short s16x4 __attribute__((ext_vector_type(4)));
#define LAS __attribute__((address_space(3)))

constexpr int DM = 640, NH = 32, SK = 4096, DH = 128, WIN = 1024, HD = NH * DH;
constexpr int AUX_SC1 = 16;
constexpr unsigned RSRC_W3 = 0x00020000u;
constexpr int WS_E = 0;
constexpr int WS_ML = 4096;
constexpr int WS_PO = 16384;
constexpr int WS_Y = WS_PO + 32 * 8 * 1024;
constexpr int WS_END = WS_Y + 2 * 10 * 32 * 512;
constexpr int WS_STAMP = 1 << 20;
constexpr unsigned SPIN_CAP = 1u << 16;
constexpr int L_QP = 0;
constexpr int L_OP = 4096;
constexpr int L_XB = 8192;
constexpr int L_WM = 10240;
constexpr int L_WL = 10272;
constexpr int L_END = 10304;

struct Params {
  const unsigned char* x[2];
  const unsigned char* k[2];
  const unsigned char* v[2];
  const unsigned char* wq[2];
  const unsigned char* wo[2];
  unsigned char* out;
  unsigned char* ws;
};

__device__ __forceinline__ __amdgpu_buffer_rsrc_t mk(const unsigned char* p, unsigned bytes) {
  return __builtin_amdgcn_make_buffer_rsrc((void*)p, 0, (int)bytes, RSRC_W3);
}
template <int AUX> __device__ __forceinline__ v4u ld128(__amdgpu_buffer_rsrc_t rs, int voff, int soff) {
  return __builtin_amdgcn_raw_buffer_load_b128(rs, voff, soff, AUX);
}
__device__ __forceinline__ f32x4 mfma32(v4u a, v4u b, f32x4 c) {
  return __builtin_amdgcn_mfma_f32_16x16x32_bf16(__builtin_bit_cast(bf16x8, a), __builtin_bit_cast(bf16x8, b), c, 0, 0, 0);
}
__device__ __forceinline__ f32x4 mfma16(v2u a, v2u b, f32x4 c) {
  return __builtin_amdgcn_mfma_f32_16x16x16bf16_1k(__builtin_bit_cast(s16x4, a), __builtin_bit_cast(s16x4, b), c, 0, 0, 0);
}
__device__ __forceinline__ unsigned pk_lo(unsigned r0, unsigned r1) { return __builtin_amdgcn_perm(r1, r0, 0x05040100u); }
__device__ __forceinline__ unsigned pk_hi(unsigned r0, unsigned r1) { return __builtin_amdgcn_perm(r1, r0, 0x07060302u); }
__device__ __forceinline__ unsigned cvtpk(float lo, float hi) {
  f32x2 f = {lo, hi};
  return __builtin_bit_cast(unsigned, __builtin_convertvector(f, bf16x2));
}
__device__ __forceinline__ float bflo(unsigned u) { return __builtin_bit_cast(float, u << 16); }
__device__ __forceinline__ float bfhi(unsigned u) { return __builtin_bit_cast(float, u & 0xffff0000u); }
template <int CTRL> __device__ __forceinline__ float dpp(float x) {
  return __builtin_bit_cast(float, __builtin_amdgcn_mov_dpp(__builtin_bit_cast(int, x), CTRL, 0xf, 0xf, true));
}
__device__ __forceinline__ float xrow_max(float x) {
  auto s = __builtin_amdgcn_permlane16_swap(__float_as_uint(x), __float_as_uint(x), false, false);
  x = fmaxf(__uint_as_float(s[0]), __uint_as_float(s[1]));
  auto t = __builtin_amdgcn_permlane32_swap(__float_as_uint(x), __float_as_uint(x), false, false);
  return fmaxf(__uint_as_float(t[0]), __uint_as_float(t[1]));
}
__device__ __forceinline__ float xrow_sum(float x) {
  auto s = __builtin_amdgcn_permlane16_swap(__float_as_uint(x), __float_as_uint(x), false, false);
  x = __uint_as_float(s[0]) + __uint_as_float(s[1]);
  auto t = __builtin_amdgcn_permlane32_swap(__float_as_uint(x), __float_as_uint(x), false, false);
  return __uint_as_float(t[0]) + __uint_as_float(t[1]);
}
#define SB() __builtin_amdgcn_sched_barrier(0)
#define WG_BARRIER() asm volatile("s_waitcnt lgkmcnt(0)\n\ts_barrier" ::: "memory")
#define LDS_WAIT() asm volatile("s_waitcnt lgkmcnt(0)" ::: "memory")
#define STAMP(i) do { if ((i) == 0 || (i) == TELE) { SB(); tst[(i) == 0 ? 0 : 1] = (unsigned)__builtin_amdgcn_s_memrealtime(); SB(); } } while (0)

__device__ __forceinline__ void rows8_mfma(const v4u* r, v4u a, f32x4* acc) {
#pragma unroll
  for (int d = 0; d < 4; ++d) {
    v4u lo = {pk_lo(r[0][d], r[1][d]), pk_lo(r[2][d], r[3][d]), pk_lo(r[4][d], r[5][d]), pk_lo(r[6][d], r[7][d])};
    v4u hi = {pk_hi(r[0][d], r[1][d]), pk_hi(r[2][d], r[3][d]), pk_hi(r[4][d], r[5][d]), pk_hi(r[6][d], r[7][d])};
    acc[2 * d] = mfma32(a, lo, acc[2 * d]);
    acc[2 * d + 1] = mfma32(a, hi, acc[2 * d + 1]);
  }
}
__device__ __forceinline__ void rows4_mfma(const v4u* r, v2u a, f32x4* acc) {
#pragma unroll
  for (int d = 0; d < 4; ++d) {
    v2u lo = {pk_lo(r[0][d], r[1][d]), pk_lo(r[2][d], r[3][d])};
    v2u hi = {pk_hi(r[0][d], r[1][d]), pk_hi(r[2][d], r[3][d])};
    acc[2 * d] = mfma16(a, lo, acc[2 * d]);
    acc[2 * d + 1] = mfma16(a, hi, acc[2 * d + 1]);
  }
}

template <int BR>
__device__ __forceinline__ void body(const Params& p, LAS unsigned char* L, int hg, int j, int w, int lane) {
  constexpr int NT = BR ? 3 : 2;
  constexpr int NP = BR ? 6 : 2;
  constexpr int J0 = BR ? 0 : 6;
  const int c = lane & 15, g = lane >> 4;
  unsigned tst[2] = {0u, 0u};
  STAMP(0);
  const __amdgpu_buffer_rsrc_t rws = mk(p.ws, WS_END);
  const unsigned e_raw = __builtin_amdgcn_raw_buffer_load_b32(rws, 0, WS_E + 256 * BR, AUX_SC1);

  if (TELE == 12) { asm volatile("s_waitcnt vmcnt(0)" ::: "memory"); STAMP(12); }
  if (w == 0 && lane == 0) __builtin_amdgcn_raw_buffer_store_b32(tst[0], mk(p.ws, 1 << 21), blockIdx.x * 4, WS_STAMP, AUX_SC1);
  int tile0, nt;
  if (BR) {
    const int cu0 = j < 2 ? 22 * j : 44 + 21 * (j - 2);
    const int n3 = j < 2 ? 6 : 5;
    nt = w < n3 ? 3 : 2;
    tile0 = cu0 + (w < n3 ? 3 * w : 3 * n3 + 2 * (w - n3));
  } else {
    nt = 2;
    tile0 = (SK - WIN) / 32 + (j - 6) * 16 + 2 * w;
  }
  int cb = -1, ru = -1;
  if (BR) {
    if (j < 2) { if (w == 7) cb = j; if (w == 6) ru = j == 0 ? hg : ((hg & 3) == 0 ? 32 + (hg >> 2) : -1); }
    else if (w >= 6) cb = 2 + 2 * (j - 2) + (w - 6);
  } else {
    if (w >= 3) cb = 5 * (j - 6) + (w - 3);
    if (w == 2) ru = j == 6 ? hg : ((hg & 3) == 0 ? 32 + (hg >> 2) : -1);
  }
  const size_t kvoff = ((size_t)hg * SK + (size_t)tile0 * 32) * 256;
  const __amdgpu_buffer_rsrc_t rk = mk(p.k[BR] + kvoff, nt * 8192), rv = mk(p.v[BR] + kvoff, nt * 8192);
  const __amdgpu_buffer_rsrc_t rq = mk(p.wq[BR], DM * HD * 2), rx = mk(p.x[BR], DM * 2);

  const v4u xa0 = ld128<0>(rx, g * 16, 160 * w), xa1 = ld128<0>(rx, g * 16, 160 * w + 64);
  const v2u xa2 = __builtin_amdgcn_raw_buffer_load_b64(rx, g * 8, 160 * w + 128, 0);
  v4u wq[20], kk[8 * NT], vv[8 * NT];
  {
    const int sb = 80 * w * (HD * 2) + hg * 256;
    const int vo8 = g * 8 * (HD * 2) + c * 16, vo4 = g * 4 * (HD * 2) + c * 16;
#pragma unroll
    for (int i = 0; i < 16; ++i) wq[i] = ld128<AUX_WQ>(rq, vo8, sb + ((i >> 3) * 32 + (i & 7)) * (HD * 2));
#pragma unroll
    for (int i = 0; i < 4; ++i) wq[16 + i] = ld128<AUX_WQ>(rq, vo4, sb + (64 + i) * (HD * 2));
  }
  const int kvo = (c >> 1) * 256 + (c & 1) * 64 + g * 16;
#pragma unroll
  for (int i = 0; i < 8 * NT; ++i) kk[i] = ld128<AUX_KV>(rk, kvo, (i >> 1) * 2048 + (i & 1) * 128);
  const int vvo = g * 512 + c * 16;
#define VLOAD(i) vv[i] = ld128<AUX_KV>(rv, vvo, ((i) >> 3) * 8192 + (((i) & 7) >> 1) * 2048 + ((i) & 1) * 256)
  constexpr int V0 = BR ? 4 : 12;
#pragma unroll
  for (int i = 0; i < V0; ++i) VLOAD(i);
  SB();
  STAMP(1);
  {
    f32x4 acc[8];
#pragma unroll
    for (int i = 0; i < 8; ++i) acc[i] = f32x4{0.f, 0.f, 0.f, 0.f};
    rows8_mfma(wq, xa0, acc);
    SB();
#pragma unroll
    for (int i = V0; i < V0 + 8 && i < 8 * NT; ++i) VLOAD(i);
    SB();
    rows8_mfma(wq + 8, xa1, acc);
    SB();
#pragma unroll
    for (int i = V0 + 8; i < V0 + 16 && i < 8 * NT; ++i) VLOAD(i);
    SB();
    rows4_mfma(wq + 16, xa2, acc);
    SB();
#pragma unroll
    for (int i = V0 + 16; i < 8 * NT; ++i) VLOAD(i);
    if (g == 0) {
      LAS f32x4* qp = (LAS f32x4*)(L + L_QP + w * 512 + c * 32);
      qp[0] = f32x4{acc[0][0], acc[1][0], acc[2][0], acc[3][0]};
      qp[1] = f32x4{acc[4][0], acc[5][0], acc[6][0], acc[7][0]};
    }
  }
  STAMP(2);
  WG_BARRIER();
  v4u qf0, qf1;
  {
    f32x2 s = {0.f, 0.f};
#pragma unroll
    for (int ww = 0; ww < 8; ++ww) s += *(LAS f32x2*)(L + L_QP + ww * 512 + lane * 8);
    *(LAS unsigned*)(L + L_XB + w * 256 + lane * 4) = cvtpk(s[0], s[1]);
    LDS_WAIT();
    qf0 = *(LAS v4u*)(L + L_XB + w * 256 + (c & 1) * 64 + g * 16);
    qf1 = *(LAS v4u*)(L + L_XB + w * 256 + 128 + (c & 1) * 64 + g * 16);
  }
  STAMP(3);
  float sc[8 * NT];
  const __amdgpu_buffer_rsrc_t ro = mk(p.wo[BR], cb >= 0 ? HD * DM * 2 : 0);
  v4u wo[16];
  const int wovo = ((c >> 3) * 32 + g * 8) * (DM * 2) + (c & 7) * 16;
  const int wosb = hg * 128 * (DM * 2) + cb * 128;
#pragma unroll
  for (int t = 0; t < 4 * NT; ++t) {
    f32x4 d = {0.f, 0.f, 0.f, 0.f};
    d = mfma32(kk[2 * t], qf0, d);
    d = mfma32(kk[2 * t + 1], qf1, d);
    const float s0 = d[0] + dpp<0xB1>(d[1]), s1 = d[2] + dpp<0xB1>(d[3]);
    const unsigned u = cvtpk(s0, s1);
    const unsigned u2 = cvtpk(bflo(u) * 0.08838834764831845f, bfhi(u) * 0.08838834764831845f);
    sc[2 * t] = bflo(u2);
    sc[2 * t + 1] = bfhi(u2);
  }
  SB();
#pragma unroll
  for (int i = 0; i < 16; ++i) wo[i] = ld128<AUX_WO>(ro, wovo, wosb + ((i >> 3) * 64 + (i & 7)) * (DM * 2));
  SB();
  if (BR && nt == 2) {
#pragma unroll
    for (int i = 16; i < 8 * NT; ++i) sc[i] = -INFINITY;
  }
  float mx = sc[0];
#pragma unroll
  for (int i = 1; i < 8 * NT; ++i) mx = fmaxf(mx, sc[i]);
  mx = xrow_max(mx);
  if (lane == 0) *(LAS float*)(L + L_WM + w * 4) = mx;
  STAMP(4);
  WG_BARRIER();
  float M;
  {
    const f32x4 a = *(LAS f32x4*)(L + L_WM), b = *(LAS f32x4*)(L + L_WM + 16);
    M = fmaxf(fmaxf(fmaxf(a[0], a[1]), fmaxf(a[2], a[3])), fmaxf(fmaxf(b[0], b[1]), fmaxf(b[2], b[3])));
  }
  float lsum = 0.f;
  v4u pa[NT];
#pragma unroll
  for (int T = 0; T < NT; ++T) {
#pragma unroll
    for (int t = 0; t < 4; ++t) {
      const float p0 = __builtin_amdgcn_exp2f((sc[8 * T + 2 * t] - M) * 1.4426950408889634f);
      const float p1 = __builtin_amdgcn_exp2f((sc[8 * T + 2 * t + 1] - M) * 1.4426950408889634f);
      lsum += p0; lsum += p1;
      pa[T][t] = cvtpk(p0, p1);
    }
  }
  lsum = xrow_sum(lsum);
  if (lane == 0) *(LAS float*)(L + L_WL + w * 4) = lsum;
  STAMP(5);
  {
    f32x4 acc[8];
#pragma unroll
    for (int i = 0; i < 8; ++i) acc[i] = f32x4{0.f, 0.f, 0.f, 0.f};
#pragma unroll
    for (int T = 0; T < NT; ++T) rows8_mfma(vv + 8 * T, pa[T], acc);
    if (g == 0) {
      LAS f32x4* op = (LAS f32x4*)(L + L_OP + w * 512 + c * 32);
      op[0] = f32x4{acc[0][0], acc[1][0], acc[2][0], acc[3][0]};
      op[1] = f32x4{acc[4][0], acc[5][0], acc[6][0], acc[7][0]};
    }
  }
  STAMP(6);
  const unsigned tag = __builtin_amdgcn_readfirstlane(e_raw) + 1u;
  WG_BARRIER();
  STAMP(7);
  if (w == 0) {
    f32x2 s = {0.f, 0.f};
#pragma unroll
    for (int ww = 0; ww < 8; ++ww) s += *(LAS f32x2*)(L + L_OP + ww * 512 + lane * 8);
    const v4u u = {__float_as_uint(s[0]), tag, __float_as_uint(s[1]), tag};
    __builtin_amdgcn_raw_buffer_store_b128(u, rws, lane * 16, WS_PO + (hg * 8 + j) * 1024, AUX_SC1);
  } else if (w == 1) {
    const f32x4 a = *(LAS f32x4*)(L + L_WL), b = *(LAS f32x4*)(L + L_WL + 16);
    const float l = ((a[0] + a[1]) + (a[2] + a[3])) + ((b[0] + b[1]) + (b[2] + b[3]));
    const v4u u = {__float_as_uint(M), tag, __float_as_uint(l), tag};
    if (lane == 0) __builtin_amdgcn_raw_buffer_store_b128(u, rws, 0, WS_ML + (BR * 32 + hg) * 128 + j * 16, AUX_SC1);
  } else if (cb >= 0) {
    v4u wf[16];
#pragma unroll
    for (int t = 0; t < 2; ++t)
#pragma unroll
      for (int d = 0; d < 4; ++d) {
        const v4u* r = wo + 8 * t;
        wf[8 * t + 2 * d] = v4u{pk_lo(r[0][d], r[1][d]), pk_lo(r[2][d], r[3][d]), pk_lo(r[4][d], r[5][d]), pk_lo(r[6][d], r[7][d])};
        wf[8 * t + 2 * d + 1] = v4u{pk_hi(r[0][d], r[1][d]), pk_hi(r[2][d], r[3][d]), pk_hi(r[4][d], r[5][d]), pk_hi(r[6][d], r[7][d])};
      }
    SB();
    v4u po[NP], ml;
    const int mlv = (lane < NP ? lane : 0) * 16;
    for (unsigned spins = 0;; ++spins) {
      ml = ld128<AUX_SC1>(rws, mlv, WS_ML + (BR * 32 + hg) * 128 + J0 * 16);
#pragma unroll
      for (int s = 0; s < NP; ++s) po[s] = ld128<AUX_SC1>(rws, lane * 16, WS_PO + (hg * 8 + J0 + s) * 1024);
      bool ok = ml[1] == tag && ml[3] == tag;
#pragma unroll
      for (int s = 0; s < NP; ++s) ok = ok && po[s][1] == tag && po[s][3] == tag;
      if (__all(ok) || spins > SPIN_CAP) break;
      __builtin_amdgcn_s_sleep(2);
    }
    STAMP(8);
    float ms[NP], Mg = -INFINITY;
#pragma unroll
    for (int s = 0; s < NP; ++s) { ms[s] = __uint_as_float(__builtin_amdgcn_readlane(ml[0], s)); Mg = fmaxf(Mg, ms[s]); }
    float Lg = 0.f, o0 = 0.f, o1 = 0.f;
#pragma unroll
    for (int s = 0; s < NP; ++s) {
      const float ws_ = __builtin_amdgcn_exp2f((ms[s] - Mg) * 1.4426950408889634f);
      Lg = fmaf(ws_, __uint_as_float(__builtin_amdgcn_readlane(ml[2], s)), Lg);
      o0 = fmaf(ws_, __uint_as_float(po[s][0]), o0);
      o1 = fmaf(ws_, __uint_as_float(po[s][2]), o1);
    }
    const float inv = 1.0f / Lg;
    *(LAS unsigned*)(L + L_XB + w * 256 + lane * 4) = cvtpk(o0 * inv, o1 * inv);
    LDS_WAIT();
    const v4u oa0 = *(LAS v4u*)(L + L_XB + w * 256 + (c & 1) * 64 + g * 16);
    const v4u oa1 = *(LAS v4u*)(L + L_XB + w * 256 + 128 + (c & 1) * 64 + g * 16);
    float y[8];
#pragma unroll
    for (int jj = 0; jj < 8; ++jj) {
      f32x4 d = {0.f, 0.f, 0.f, 0.f};
      d = mfma32(oa0, wf[jj], d);
      d = mfma32(oa1, wf[8 + jj], d);
      y[jj] = d[0] + dpp<0x128>(d[1]);
    }
    const float y0 = g == 0 ? y[0] : g == 1 ? y[2] : g == 2 ? y[4] : y[6];
    const float y1 = g == 0 ? y[1] : g == 1 ? y[3] : g == 2 ? y[5] : y[7];
    const v4u u = {__float_as_uint(y0), tag, __float_as_uint(y1), tag};
    if (c < 8) __builtin_amdgcn_raw_buffer_store_b128(u, rws, c * 64 + g * 16, WS_Y + ((BR * 10 + cb) * 32 + hg) * 512, AUX_SC1);
    STAMP(9);
  } else if (ru >= 0) {
    v4u yy[4];
    const int vo = (lane >> 3) * 512 + (lane & 7) * 16;
    const int so = WS_Y + (BR * 10 + (ru >> 2)) * 32 * 512 + (ru & 3) * 128;
    for (unsigned spins = 0;; ++spins) {
#pragma unroll
      for (int i = 0; i < 4; ++i) yy[i] = ld128<AUX_SC1>(rws, vo, so + i * 8 * 512);
      bool ok = true;
#pragma unroll
      for (int i = 0; i < 4; ++i) ok = ok && yy[i][1] == tag && yy[i][3] == tag;
      if (__all(ok) || spins > SPIN_CAP) break;
      __builtin_amdgcn_s_sleep(2);
    }
    STAMP(10);
    float a0 = (__uint_as_float(yy[0][0]) + __uint_as_float(yy[1][0])) + (__uint_as_float(yy[2][0]) + __uint_as_float(yy[3][0]));
    float a1 = (__uint_as_float(yy[0][2]) + __uint_as_float(yy[1][2])) + (__uint_as_float(yy[2][2]) + __uint_as_float(yy[3][2]));
    a0 += dpp<0x128>(a0); a1 += dpp<0x128>(a1);
    a0 = xrow_sum(a0); a1 = xrow_sum(a1);
    if (lane < 8) *(unsigned*)(p.out + (BR * DM + ru * 16 + lane * 2) * 2) = cvtpk(a0, a1);
    if (ru == 0 && lane == 0) __builtin_amdgcn_raw_buffer_store_b32(tag, rws, 0, WS_E + 256 * BR, AUX_SC1);
    STAMP(11);
    if (BR == 1 && ru == 0) {
      const unsigned t2 = (unsigned)__builtin_amdgcn_s_memrealtime();
      const v4u st4 = ld128<AUX_SC1>(mk(p.ws, 1 << 21), lane * 16, WS_STAMP);
      int mn = 1 << 30, mx = -(1 << 30);
      for (int i = 0; i < 4; ++i) { const int dd = (int)(st4[i] - tst[0]); mn = min(mn, dd); mx = max(mx, dd); }
      for (int o = 32; o > 0; o >>= 1) { mn = min(mn, __shfl_xor(mn, o)); mx = max(mx, __shfl_xor(mx, o)); }
      unsigned d = (unsigned)(TELE == 20 ? -mn : mx) * 10u;
      d = __builtin_amdgcn_readfirstlane(d);
      if (d > 100000u) d = 100000u;
      while ((unsigned)__builtin_amdgcn_s_memrealtime() - t2 < d) __builtin_amdgcn_s_sleep(4);
    }
  }
}

__global__ void __launch_bounds__(512, 2) fused_decode_kernel(Params p) {
  __shared__ __attribute__((aligned(16))) unsigned char smem[L_END];
  LAS unsigned char* L = (LAS unsigned char*)smem;
  const int b = blockIdx.x, lane = threadIdx.x & 63, w = __builtin_amdgcn_readfirstlane(threadIdx.x >> 6);
  const int hg = (b & 7) * 4 + (b >> 6), j = (b >> 3) & 7;
  if (j < 6) body<1>(p, L, hg, j, w, lane);
  else body<0>(p, L, hg, j, w, lane);
}

extern "C" void kernel_launch(void* const* d_in, const int* in_sizes, int n_in, void* d_out, int out_size, void* d_ws, size_t ws_size,
                              hipStream_t stream) {
  Params p;
  for (int br = 0; br < 2; ++br) {
    p.x[br] = (const unsigned char*)d_in[5 * br + 0];
    p.k[br] = (const unsigned char*)d_in[5 * br + 1];
    p.v[br] = (const unsigned char*)d_in[5 * br + 2];
    p.wq[br] = (const unsigned char*)d_in[5 * br + 3];
    p.wo[br] = (const unsigned char*)d_in[5 * br + 4];
  }
  p.out = (unsigned char*)d_out;
  p.ws = (unsigned char*)d_ws;
  fused_decode_kernel<<<256, 512, 0, stream>>>(p);
}
```

```cpp
#define WHICH 0
#define TELE 11
#include <hip/hip_runtime.h>
#include <stdint.h>

#define AUX_KV 2
#define AUX_WQ 2
#define AUX_WO 2
#define STAMPS 0

typedef unsigned v4u __attribute__((ext_vector_type(4)));
typedef unsigned v2u __attribute__((ext_vector_type(2)));
typedef float f32x4 __attribute__((ext_vector_type(4)));
typedef float f32x2 __attribute__((ext_vector_type(2)));
typedef __bf16 bf16x8 __attribute__((ext_vector_type(8)));
typedef __bf16 bf16x2 __attribute__((ext_vector_type(2)));
typedef short s16x4 __attribute__((ext_vector_type(4)));
#define LAS __attribute__((address_space(3)))

constexpr int DM = 640, NH = 32, SK = 4096, DH = 128, WIN = 1024, HD = NH * DH;
constexpr int AUX_SC1 = 16;
constexpr unsigned RSRC_W3 = 0x00020000u;
constexpr int WS_E = 0;
constexpr int WS_ML = 4096;
constexpr int WS_PO = 16384;
constexpr int WS_Y = WS_PO + 32 * 8 * 1024;
constexpr int WS_END = WS_Y + 2 * 10 * 32 * 512;
constexpr int WS_STAMP = 1 << 20;
constexpr unsigned SPIN_CAP = 1u << 16;
constexpr int L_QP = 0;
constexpr int L_OP = 4096;
constexpr int L_XB = 8192;
constexpr int L_WM = 10240;
constexpr int L_WL = 10272;
constexpr int L_END = 10304;

struct Params {
  const unsigned char* x[2];
  const unsigned char* k[2];
  const unsigned char* v[2];
  const unsigned char* wq[2];
  const unsigned char* wo[2];
  unsigned char* out;
  unsigned char* ws;
};

__device__ __forceinline__ __amdgpu_buffer_rsrc_t mk(const unsigned char* p, unsigned bytes) {
  return __builtin_amdgcn_make_buffer_rsrc((void*)p, 0, (int)bytes, RSRC_W3);
}
template <int AUX> __device__ __forceinline__ v4u ld128(__amdgpu_buffer_rsrc_t rs, int voff, int soff) {
  return __builtin_amdgcn_raw_buffer_load_b128(rs, voff, soff, AUX);
}
__device__ __forceinline__ f32x4 mfma32(v4u a, v4u b, f32x4 c) {
  return __builtin_amdgcn_mfma_f32_16x16x32_bf16(__builtin_bit_cast(bf16x8, a), __builtin_bit_cast(bf16x8, b), c, 0, 0, 0);
}
__device__ __forceinline__ f32x4 mfma16(v2u a, v2u b, f32x4 c) {
  return __builtin_amdgcn_mfma_f32_16x16x16bf16_1k(__builtin_bit_cast(s16x4, a), __builtin_bit_cast(s16x4, b), c, 0, 0, 0);
}
__device__ __forceinline__ unsigned pk_lo(unsigned r0, unsigned r1) { return __builtin_amdgcn_perm(r1, r0, 0x05040100u); }
__device__ __forceinline__ unsigned pk_hi(unsigned r0, unsigned r1) { return __builtin_amdgcn_perm(r1, r0, 0x07060302u); }
__device__ __forceinline__ unsigned cvtpk(float lo, float hi) {
  f32x2 f = {lo, hi};
  return __builtin_bit_cast(unsigned, __builtin_convertvector(f, bf16x2));
}
__device__ __forceinline__ float bflo(unsigned u) { return __builtin_bit_cast(float, u << 16); }
__device__ __forceinline__ float bfhi(unsigned u) { return __builtin_bit_cast(float, u & 0xffff0000u); }
template <int CTRL> __device__ __forceinline__ float dpp(float x) {
  return __builtin_bit_cast(float, __builtin_amdgcn_mov_dpp(__builtin_bit_cast(int, x), CTRL, 0xf, 0xf, true));
}
__device__ __forceinline__ float xrow_max(float x) {
  auto s = __builtin_amdgcn_permlane16_swap(__float_as_uint(x), __float_as_uint(x), false, false);
  x = fmaxf(__uint_as_float(s[0]), __uint_as_float(s[1]));
  auto t = __builtin_amdgcn_permlane32_swap(__float_as_uint(x), __float_as_uint(x), false, false);
  return fmaxf(__uint_as_float(t[0]), __uint_as_float(t[1]));
}
__device__ __forceinline__ float xrow_sum(float x) {
  auto s = __builtin_amdgcn_permlane16_swap(__float_as_uint(x), __float_as_uint(x), false, false);
  x = __uint_as_float(s[0]) + __uint_as_float(s[1]);
  auto t = __builtin_amdgcn_permlane32_swap(__float_as_uint(x), __float_as_uint(x), false, false);
  return __uint_as_float(t[0]) + __uint_as_float(t[1]);
}
#define SB() __builtin_amdgcn_sched_barrier(0)
#define WG_BARRIER() asm volatile("s_waitcnt lgkmcnt(0)\n\ts_barrier" ::: "memory")
#define LDS_WAIT() asm volatile("s_waitcnt lgkmcnt(0)" ::: "memory")
#define STAMP(i) do { if ((i) == 0 || (i) == TELE) { SB(); tst[(i) == 0 ? 0 : 1] = (unsigned)__builtin_amdgcn_s_memrealtime(); SB(); } } while (0)

__device__ __forceinline__ void rows8_mfma(const v4u* r, v4u a, f32x4* acc) {
#pragma unroll
  for (int d = 0; d < 4; ++d) {
    v4u lo = {pk_lo(r[0][d], r[1][d]), pk_lo(r[2][d], r[3][d]), pk_lo(r[4][d], r[5][d]), pk_lo(r[6][d], r[7][d])};
    v4u hi = {pk_hi(r[0][d], r[1][d]), pk_hi(r[2][d], r[3][d]), pk_hi(r[4][d], r[5][d]), pk_hi(r[6][d], r[7][d])};
    acc[2 * d] = mfma32(a, lo, acc[2 * d]);
    acc[2 * d + 1] = mfma32(a, hi, acc[2 * d + 1]);
  }
}
__device__ __forceinline__ void rows4_mfma(const v4u* r, v2u a, f32x4* acc) {
#pragma unroll
  for (int d = 0; d < 4; ++d) {
    v2u lo = {pk_lo(r[0][d], r[1][d]), pk_lo(r[2][d], r[3][d])};
    v2u hi = {pk_hi(r[0][d], r[1][d]), pk_hi(r[2][d], r[3][d])};
    acc[2 * d] = mfma16(a, lo, acc[2 * d]);
    acc[2 * d + 1] = mfma16(a, hi, acc[2 * d + 1]);
  }
}

template <int BR>
__device__ __forceinline__ void body(const Params& p, LAS unsigned char* L, int hg, int j, int w, int lane) {
  constexpr int NT = BR ? 3 : 2;
  constexpr int NP = BR ? 6 : 2;
  constexpr int J0 = BR ? 0 : 6;
  const int c = lane & 15, g = lane >> 4;
  unsigned tst[2] = {0u, 0u};
  STAMP(0);
  const __amdgpu_buffer_rsrc_t rws = mk(p.ws, WS_END);
  const unsigned e_raw = __builtin_amdgcn_raw_buffer_load_b32(rws, 0, WS_E + 256 * BR, AUX_SC1);

  if (TELE == 12) { asm volatile("s_waitcnt vmcnt(0)" ::: "memory"); STAMP(12); }
  int tile0, nt;
  if (BR) {
    const int cu0 = j < 2 ? 22 * j : 44 + 21 * (j - 2);
    const int n3 = j < 2 ? 6 : 5;
    nt = w < n3 ? 3 : 2;
    tile0 = cu0 + (w < n3 ? 3 * w : 3 * n3 + 2 * (w - n3));
  } else {
    nt = 2;
    tile0 = (SK - WIN) / 32 + (j - 6) * 16 + 2 * w;
  }
  int cb = -1, ru = -1;
  if (BR) {
    if (j < 2) { if (w == 7) cb = j; if (w == 6) ru = j == 0 ? hg : ((hg & 3) == 0 ? 32 + (hg >> 2) : -1); }
    else if (w >= 6) cb = 2 + 2 * (j - 2) + (w - 6);
  } else {
    if (w >= 3) cb = 5 * (j - 6) + (w - 3);
    if (w == 2) ru = j == 6 ? hg : ((hg & 3) == 0 ? 32 + (hg >> 2) : -1);
  }
  const size_t kvoff = ((size_t)hg * SK + (size_t)tile0 * 32) * 256;
  const __amdgpu_buffer_rsrc_t rk = mk(p.k[BR] + kvoff, nt * 8192), rv = mk(p.v[BR] + kvoff, nt * 8192);
  const __amdgpu_buffer_rsrc_t rq = mk(p.wq[BR], DM * HD * 2), rx = mk(p.x[BR], DM * 2);

  const v4u xa0 = ld128<0>(rx, g * 16, 160 * w), xa1 = ld128<0>(rx, g * 16, 160 * w + 64);
  const v2u xa2 = __builtin_amdgcn_raw_buffer_load_b64(rx, g * 8, 160 * w + 128, 0);
  v4u wq[20], kk[8 * NT], vv[8 * NT];
  {
    const int sb = 80 * w * (HD * 2) + hg * 256;
    const int vo8 = g * 8 * (HD * 2) + c * 16, vo4 = g * 4 * (HD * 2) + c * 16;
#pragma unroll
    for (int i = 0; i < 16; ++i) wq[i] = ld128<AUX_WQ>(rq, vo8, sb + ((i >> 3) * 32 + (i & 7)) * (HD * 2));
#pragma unroll
    for (int i = 0; i < 4; ++i) wq[16 + i] = ld128<AUX_WQ>(rq, vo4, sb + (64 + i) * (HD * 2));
  }
  const int kvo = (c >> 1) * 256 + (c & 1) * 64 + g * 16;
#pragma unroll
  for (int i = 0; i < 8 * NT; ++i) kk[i] = ld128<AUX_KV>(rk, kvo, (i >> 1) * 2048 + (i & 1) * 128);
  const int vvo = g * 512 + c * 16;
#define VLOAD(i) vv[i] = ld128<AUX_KV>(rv, vvo, ((i) >> 3) * 8192 + (((i) & 7) >> 1) * 2048 + ((i) & 1) * 256)
  constexpr int V0 = BR ? 4 : 12;
#pragma unroll
  for (int i = 0; i < V0; ++i) VLOAD(i);
  SB();
  STAMP(1);
  {
    f32x4 acc[8];
#pragma unroll
    for (int i = 0; i < 8; ++i) acc[i] = f32x4{0.f, 0.f, 0.f, 0.f};
    rows8_mfma(wq, xa0, acc);
    SB();
#pragma unroll
    for (int i = V0; i < V0 + 8 && i < 8 * NT; ++i) VLOAD(i);
    SB();
    rows8_mfma(wq + 8, xa1, acc);
    SB();
#pragma unroll
    for (int i = V0 + 8; i < V0 + 16 && i < 8 * NT; ++i) VLOAD(i);
    SB();
    rows4_mfma(wq + 16, xa2, acc);
    SB();
#pragma unroll
    for (int i = V0 + 16; i < 8 * NT; ++i) VLOAD(i);
    if (g == 0) {
      LAS f32x4* qp = (LAS f32x4*)(L + L_QP + w * 512 + c * 32);
      qp[0] = f32x4{acc[0][0], acc[1][0], acc[2][0], acc[3][0]};
      qp[1] = f32x4{acc[4][0], acc[5][0], acc[6][0], acc[7][0]};
    }
  }
  STAMP(2);
  WG_BARRIER();
  v4u qf0, qf1;
  {
    f32x2 s = {0.f, 0.f};
#pragma unroll
    for (int ww = 0; ww < 8; ++ww) s += *(LAS f32x2*)(L + L_QP + ww * 512 + lane * 8);
    *(LAS unsigned*)(L + L_XB + w * 256 + lane * 4) = cvtpk(s[0], s[1]);
    LDS_WAIT();
    qf0 = *(LAS v4u*)(L + L_XB + w * 256 + (c & 1) * 64 + g * 16);
    qf1 = *(LAS v4u*)(L + L_XB + w * 256 + 128 + (c & 1) * 64 + g * 16);
  }
  STAMP(3);
  float sc[8 * NT];
  const __amdgpu_buffer_rsrc_t ro = mk(p.wo[BR], cb >= 0 ? HD * DM * 2 : 0);
  v4u wo[16];
  const int wovo = ((c >> 3) * 32 + g * 8) * (DM * 2) + (c & 7) * 16;
  const int wosb = hg * 128 * (DM * 2) + cb * 128;
#pragma unroll
  for (int t = 0; t < 4 * NT; ++t) {
    f32x4 d = {0.f, 0.f, 0.f, 0.f};
    d = mfma32(kk[2 * t], qf0, d);
    d = mfma32(kk[2 * t + 1], qf1, d);
    const float s0 = d[0] + dpp<0xB1>(d[1]), s1 = d[2] + dpp<0xB1>(d[3]);
    const unsigned u = cvtpk(s0, s1);
    const unsigned u2 = cvtpk(bflo(u) * 0.08838834764831845f, bfhi(u) * 0.08838834764831845f);
    sc[2 * t] = bflo(u2);
    sc[2 * t + 1] = bfhi(u2);
  }
  SB();
#pragma unroll
  for (int i = 0; i < 16; ++i) wo[i] = ld128<AUX_WO>(ro, wovo, wosb + ((i >> 3) * 64 + (i & 7)) * (DM * 2));
  SB();
  if (BR && nt == 2) {
#pragma unroll
    for (int i = 16; i < 8 * NT; ++i) sc[i] = -INFINITY;
  }
  float mx = sc[0];
#pragma unroll
  for (int i = 1; i < 8 * NT; ++i) mx = fmaxf(mx, sc[i]);
  mx = xrow_max(mx);
  if (lane == 0) *(LAS float*)(L + L_WM + w * 4) = mx;
  STAMP(4);
  WG_BARRIER();
  float M;
  {
    const f32x4 a = *(LAS f32x4*)(L + L_WM), b = *(LAS f32x4*)(L + L_WM + 16);
    M = fmaxf(fmaxf(fmaxf(a[0], a[1]), fmaxf(a[2], a[3])), fmaxf(fmaxf(b[0], b[1]), fmaxf(b[2], b[3])));
  }
  float lsum = 0.f;
  v4u pa[NT];
#pragma unroll
  for (int T = 0; T < NT; ++T) {
#pragma unroll
    for (int t = 0; t < 4; ++t) {
      const float p0 = __builtin_amdgcn_exp2f((sc[8 * T + 2 * t] - M) * 1.4426950408889634f);
      const float p1 = __builtin_amdgcn_exp2f((sc[8 * T + 2 * t + 1] - M) * 1.4426950408889634f);
      lsum += p0; lsum += p1;
      pa[T][t] = cvtpk(p0, p1);
    }
  }
  lsum = xrow_sum(lsum);
  if (lane == 0) *(LAS float*)(L + L_WL + w * 4) = lsum;
  STAMP(5);
  {
    f32x4 acc[8];
#pragma unroll
    for (int i = 0; i < 8; ++i) acc[i] = f32x4{0.f, 0.f, 0.f, 0.f};
#pragma unroll
    for (int T = 0; T < NT; ++T) rows8_mfma(vv + 8 * T, pa[T], acc);
    if (g == 0) {
      LAS f32x4* op = (LAS f32x4*)(L + L_OP + w * 512 + c * 32);
      op[0] = f32x4{acc[0][0], acc[1][0], acc[2][0], acc[3][0]};
      op[1] = f32x4{acc[4][0], acc[5][0], acc[6][0], acc[7][0]};
    }
  }
  STAMP(6);
  const unsigned tag = __builtin_amdgcn_readfirstlane(e_raw) + 1u;
  WG_BARRIER();
  STAMP(7);
  if (w == 0) {
    f32x2 s = {0.f, 0.f};
#pragma unroll
    for (int ww = 0; ww < 8; ++ww) s += *(LAS f32x2*)(L + L_OP + ww * 512 + lane * 8);
    const v4u u = {__float_as_uint(s[0]), tag, __float_as_uint(s[1]), tag};
    __builtin_amdgcn_raw_buffer_store_b128(u, rws, lane * 16, WS_PO + (hg * 8 + j) * 1024, AUX_SC1);
  } else if (w == 1) {
    const f32x4 a = *(LAS f32x4*)(L + L_WL), b = *(LAS f32x4*)(L + L_WL + 16);
    const float l = ((a[0] + a[1]) + (a[2] + a[3])) + ((b[0] + b[1]) + (b[2] + b[3]));
    const v4u u = {__float_as_uint(M), tag, __float_as_uint(l), tag};
    if (lane == 0) __builtin_amdgcn_raw_buffer_store_b128(u, rws, 0, WS_ML + (BR * 32 + hg) * 128 + j * 16, AUX_SC1);
  } else if (cb >= 0) {
    v4u wf[16];
#pragma unroll
    for (int t = 0; t < 2; ++t)
#pragma unroll
      for (int d = 0; d < 4; ++d) {
        const v4u* r = wo + 8 * t;
        wf[8 * t + 2 * d] = v4u{pk_lo(r[0][d], r[1][d]), pk_lo(r[2][d], r[3][d]), pk_lo(r[4][d], r[5][d]), pk_lo(r[6][d], r[7][d])};
        wf[8 * t + 2 * d + 1] = v4u{pk_hi(r[0][d], r[1][d]), pk_hi(r[2][d], r[3][d]), pk_hi(r[4][d], r[5][d]), pk_hi(r[6][d], r[7][d])};
      }
    SB();
    v4u po[NP], ml;
    const int mlv = (lane < NP ? lane : 0) * 16;
    for (unsigned spins = 0;; ++spins) {
      ml = ld128<AUX_SC1>(rws, mlv, WS_ML + (BR * 32 + hg) * 128 + J0 * 16);
#pragma unroll
      for (int s = 0; s < NP; ++s) po[s] = ld128<AUX_SC1>(rws, lane * 16, WS_PO + (hg * 8 + J0 + s) * 1024);
      bool ok = ml[1] == tag && ml[3] == tag;
#pragma unroll
      for (int s = 0; s < NP; ++s) ok = ok && po[s][1] == tag && po[s][3] == tag;
      if (__all(ok) || spins > SPIN_CAP) break;
      __builtin_amdgcn_s_sleep(2);
    }
    STAMP(8);
    float ms[NP], Mg = -INFINITY;
#pragma unroll
    for (int s = 0; s < NP; ++s) { ms[s] = __uint_as_float(__builtin_amdgcn_readlane(ml[0], s)); Mg = fmaxf(Mg, ms[s]); }
    float Lg = 0.f, o0 = 0.f, o1 = 0.f;
#pragma unroll
    for (int s = 0; s < NP; ++s) {
      const float ws_ = __builtin_amdgcn_exp2f((ms[s] - Mg) * 1.4426950408889634f);
      Lg = fmaf(ws_, __uint_as_float(__builtin_amdgcn_readlane(ml[2], s)), Lg);
      o0 = fmaf(ws_, __uint_as_float(po[s][0]), o0);
      o1 = fmaf(ws_, __uint_as_float(po[s][2]), o1);
    }
    const float inv = 1.0f / Lg;
    *(LAS unsigned*)(L + L_XB + w * 256 + lane * 4) = cvtpk(o0 * inv, o1 * inv);
    LDS_WAIT();
    const v4u oa0 = *(LAS v4u*)(L + L_XB + w * 256 + (c & 1) * 64 + g * 16);
    const v4u oa1 = *(LAS v4u*)(L + L_XB + w * 256 + 128 + (c & 1) * 64 + g * 16);
    float y[8];
#pragma unroll
    for (int jj = 0; jj < 8; ++jj) {
      f32x4 d = {0.f, 0.f, 0.f, 0.f};
      d = mfma32(oa0, wf[jj], d);
      d = mfma32(oa1, wf[8 + jj], d);
      y[jj] = d[0] + dpp<0x128>(d[1]);
    }
    const float y0 = g == 0 ? y[0] : g == 1 ? y[2] : g == 2 ? y[4] : y[6];
    const float y1 = g == 0 ? y[1] : g == 1 ? y[3] : g == 2 ? y[5] : y[7];
    const v4u u = {__float_as_uint(y0), tag, __float_as_uint(y1), tag};
    if (c < 8) __builtin_amdgcn_raw_buffer_store_b128(u, rws, c * 64 + g * 16, WS_Y + ((BR * 10 + cb) * 32 + hg) * 512, AUX_SC1);
    STAMP(9);
  } else if (ru >= 0) {
    v4u yy[4];
    const int vo = (lane >> 3) * 512 + (lane & 7) * 16;
    const int so = WS_Y + (BR * 10 + (ru >> 2)) * 32 * 512 + (ru & 3) * 128;
    for (unsigned spins = 0;; ++spins) {
#pragma unroll
      for (int i = 0; i < 4; ++i) yy[i] = ld128<AUX_SC1>(rws, vo, so + i * 8 * 512);
      bool ok = true;
#pragma unroll
      for (int i = 0; i < 4; ++i) ok = ok && yy[i][1] == tag && yy[i][3] == tag;
      if (__all(ok) || spins > SPIN_CAP) break;
      __builtin_amdgcn_s_sleep(2);
    }
    STAMP(10);
    float a0 = (__uint_as_float(yy[0][0]) + __uint_as_float(yy[1][0])) + (__uint_as_float(yy[2][0]) + __uint_as_float(yy[3][0]));
    float a1 = (__uint_as_float(yy[0][2]) + __uint_as_float(yy[1][2])) + (__uint_as_float(yy[2][2]) + __uint_as_float(yy[3][2]));
    a0 += dpp<0x128>(a0); a1 += dpp<0x128>(a1);
    a0 = xrow_sum(a0); a1 = xrow_sum(a1);
    if (lane < 8) *(unsigned*)(p.out + (BR * DM + ru * 16 + lane * 2) * 2) = cvtpk(a0, a1);
    if (ru == 0 && lane == 0) __builtin_amdgcn_raw_buffer_store_b32(tag, rws, 0, WS_E + 256 * BR, AUX_SC1);
    STAMP(11);
    if (BR == 1 && ru == 0) {
      if (lane == 0) { ((unsigned*)(p.ws + WS_STAMP))[1] = tst[0]; ((unsigned*)(p.ws + WS_STAMP))[2] = tst[1]; }
    }
  }
}

__global__ void __launch_bounds__(512, 2) fused_decode_kernel(Params p) {
  __shared__ __attribute__((aligned(16))) unsigned char smem[L_END];
  LAS unsigned char* L = (LAS unsigned char*)smem;
  const int b = blockIdx.x, lane = threadIdx.x & 63, w = __builtin_amdgcn_readfirstlane(threadIdx.x >> 6);
  const int hg = (b & 7) * 4 + (b >> 6), j = (b >> 3) & 7;
  if (j < 6) body<1>(p, L, hg, j, w, lane);
  else body<0>(p, L, hg, j, w, lane);
}

__global__ void k_pre(unsigned char* ws) {
  if (blockIdx.x == 0 && threadIdx.x == 0) ((unsigned*)(ws + WS_STAMP))[0] = (unsigned)__builtin_amdgcn_s_memrealtime();
}
__global__ void k_post(unsigned char* ws) {
  const unsigned t2 = (unsigned)__builtin_amdgcn_s_memrealtime();
  const unsigned* st = (const unsigned*)(ws + WS_STAMP);
  unsigned d = WHICH == 0 ? 0u : WHICH == 1 ? (st[1] - st[0]) * 10u : (t2 - st[2]) * 10u;
  if (d > 100000u) d = 100000u;
  while ((unsigned)__builtin_amdgcn_s_memrealtime() - t2 < d) __builtin_amdgcn_s_sleep(4);
}
extern "C" void kernel_launch(void* const* d_in, const int* in_sizes, int n_in, void* d_out, int out_size, void* d_ws, size_t ws_size,
                              hipStream_t stream) {
  Params p;
  for (int br = 0; br < 2; ++br) {
    p.x[br] = (const unsigned char*)d_in[5 * br + 0];
    p.k[br] = (const unsigned char*)d_in[5 * br + 1];
    p.v[br] = (const unsigned char*)d_in[5 * br + 2];
    p.wq[br] = (const unsigned char*)d_in[5 * br + 3];
    p.wo[br] = (const unsigned char*)d_in[5 * br + 4];
  }
  p.out = (unsigned char*)d_out;
  p.ws = (unsigned char*)d_ws;
  k_pre<<<8, 64, 0, stream>>>(p.ws);
  fused_decode_kernel<<<256, 512, 0, stream>>>(p);
  k_post<<<1, 64, 0, stream>>>(p.ws);
}
```

```cpp
#include <hip/hip_runtime.h>
#include <stdint.h>

#define AUX_KV 2
#define AUX_WQ 2
#define AUX_WO 2
#define PACE 4
#define POLLPOS 12
#define B1POS 4
#define QFIRST 0
#define WODELAY 0
#define STAMPS 0
#define TELE -1

typedef unsigned v4u __attribute__((ext_vector_type(4)));
typedef unsigned v2u __attribute__((ext_vector_type(2)));
typedef float f32x4 __attribute__((ext_vector_type(4)));
typedef float f32x2 __attribute__((ext_vector_type(2)));
typedef __bf16 bf16x8 __attribute__((ext_vector_type(8)));
typedef __bf16 bf16x2 __attribute__((ext_vector_type(2)));
typedef short s16x4 __attribute__((ext_vector_type(4)));
#define LAS __attribute__((address_space(3)))

constexpr int DM = 640, NH = 32, SK = 4096, DH = 128, WIN = 1024, HD = NH * DH;
constexpr int AUX_SC1 = 16;
constexpr unsigned RSRC_W3 = 0x00020000u;
constexpr int WS_E = 0;
constexpr int WS_ML = 4096;
constexpr int WS_PO = WS_ML + 32 * 1024;
constexpr int WS_Y = WS_PO + 32 * 8 * 1024;
constexpr int WS_QP = WS_Y + 2 * 10 * 32 * 512;
constexpr int WS_END = WS_QP + 32 * 8 * 1024;
constexpr int WS_STAMP = 1 << 20;
constexpr unsigned SPIN_CAP = 1u << 16;
constexpr int L_QP = 0;
constexpr int L_OP = 4096;
constexpr int L_XB = 8192;
constexpr int L_WM = 10240;
constexpr int L_WL = 10272;
constexpr int L_Q = 10304;
constexpr int L_TAG = 10560;
constexpr int L_END = 10576;

struct Params {
  const unsigned char* x[2];
  const unsigned char* k[2];
  const unsigned char* v[2];
  const unsigned char* wq[2];
  const unsigned char* wo[2];
  unsigned char* out;
  unsigned char* ws;
};

__device__ __forceinline__ __amdgpu_buffer_rsrc_t mk(const unsigned char* p, unsigned bytes) {
  return __builtin_amdgcn_make_buffer_rsrc((void*)p, 0, (int)bytes, RSRC_W3);
}
template <int AUX> __device__ __forceinline__ v4u ld128(__amdgpu_buffer_rsrc_t rs, int voff, int soff) {
  return __builtin_amdgcn_raw_buffer_load_b128(rs, voff, soff, AUX);
}
__device__ __forceinline__ f32x4 mfma32(v4u a, v4u b, f32x4 c) {
  return __builtin_amdgcn_mfma_f32_16x16x32_bf16(__builtin_bit_cast(bf16x8, a), __builtin_bit_cast(bf16x8, b), c, 0, 0, 0);
}
__device__ __forceinline__ f32x4 mfma16(v2u a, v2u b, f32x4 c) {
  return __builtin_amdgcn_mfma_f32_16x16x16bf16_1k(__builtin_bit_cast(s16x4, a), __builtin_bit_cast(s16x4, b), c, 0, 0, 0);
}
__device__ __forceinline__ unsigned pk_lo(unsigned r0, unsigned r1) { return __builtin_amdgcn_perm(r1, r0, 0x05040100u); }
__device__ __forceinline__ unsigned pk_hi(unsigned r0, unsigned r1) { return __builtin_amdgcn_perm(r1, r0, 0x07060302u); }
__device__ __forceinline__ unsigned cvtpk(float lo, float hi) {
  f32x2 f = {lo, hi};
  return __builtin_bit_cast(unsigned, __builtin_convertvector(f, bf16x2));
}
__device__ __forceinline__ float bflo(unsigned u) { return __builtin_bit_cast(float, u << 16); }
__device__ __forceinline__ float bfhi(unsigned u) { return __builtin_bit_cast(float, u & 0xffff0000u); }
template <int CTRL> __device__ __forceinline__ float dpp(float x) {
  return __builtin_bit_cast(float, __builtin_amdgcn_mov_dpp(__builtin_bit_cast(int, x), CTRL, 0xf, 0xf, true));
}
__device__ __forceinline__ float xrow_max(float x) {
  auto s = __builtin_amdgcn_permlane16_swap(__float_as_uint(x), __float_as_uint(x), false, false);
  x = fmaxf(__uint_as_float(s[0]), __uint_as_float(s[1]));
  auto t = __builtin_amdgcn_permlane32_swap(__float_as_uint(x), __float_as_uint(x), false, false);
  return fmaxf(__uint_as_float(t[0]), __uint_as_float(t[1]));
}
__device__ __forceinline__ float xrow_sum(float x) {
  auto s = __builtin_amdgcn_permlane16_swap(__float_as_uint(x), __float_as_uint(x), false, false);
  x = __uint_as_float(s[0]) + __uint_as_float(s[1]);
  auto t = __builtin_amdgcn_permlane32_swap(__float_as_uint(x), __float_as_uint(x), false, false);
  return __uint_as_float(t[0]) + __uint_as_float(t[1]);
}
constexpr int vm_enc(int n) { return (n & 0xF) | ((n >> 4) << 14) | (0x7 << 4) | (0xF << 8); }
#define SB() __builtin_amdgcn_sched_barrier(0)
#define WG_BARRIER() do { SB(); asm volatile("s_waitcnt lgkmcnt(0)\n\ts_barrier" ::: "memory"); SB(); } while (0)
#define LDS_WAIT() asm volatile("s_waitcnt lgkmcnt(0)" ::: "memory")
#define STAMP(i) do { if ((i) == 0 || (i) == TELE) { if (TELE >= 0) { SB(); tst[i] = (unsigned)__builtin_amdgcn_s_memrealtime(); SB(); } } } while (0)

__device__ __forceinline__ void rows8_mfma(const v4u* r, v4u a, f32x4* acc) {
#pragma unroll
  for (int d = 0; d < 4; ++d) {
    v4u lo = {pk_lo(r[0][d], r[1][d]), pk_lo(r[2][d], r[3][d]), pk_lo(r[4][d], r[5][d]), pk_lo(r[6][d], r[7][d])};
    v4u hi = {pk_hi(r[0][d], r[1][d]), pk_hi(r[2][d], r[3][d]), pk_hi(r[4][d], r[5][d]), pk_hi(r[6][d], r[7][d])};
    acc[2 * d] = mfma32(a, lo, acc[2 * d]);
    acc[2 * d + 1] = mfma32(a, hi, acc[2 * d + 1]);
  }
}
__device__ __forceinline__ void rows4_mfma(const v4u* r, v2u a, f32x4* acc) {
#pragma unroll
  for (int d = 0; d < 4; ++d) {
    v2u lo = {pk_lo(r[0][d], r[1][d]), pk_lo(r[2][d], r[3][d])};
    v2u hi = {pk_hi(r[0][d], r[1][d]), pk_hi(r[2][d], r[3][d])};
    acc[2 * d] = mfma16(a, lo, acc[2 * d]);
    acc[2 * d + 1] = mfma16(a, hi, acc[2 * d + 1]);
  }
}

template <int BR> struct Stream {
  static constexpr int NT = BR ? 3 : 2;
  static constexpr int NQ = BR ? 4 : 10;
  static constexpr int NK = 8 * NT;
  static constexpr int NA = NQ + NK;
  static constexpr int NALL = NA + NK;
  __amdgpu_buffer_rsrc_t rq, rk, rv;
  int qvo, qvo2, qsb, kvo, vvo, ts[3];
  v4u wq[NQ], kk[NK], vv[NK];
  static constexpr int pos_k(int i) { return NQ + i; }
  static constexpr int pos_v(int i) { return NA + i; }
  template <int N> __device__ __forceinline__ void issue() {
    if constexpr (N < NALL) {
      if constexpr (QFIRST && N < NQ) {}
      else if constexpr (QFIRST && N == NQ) __builtin_amdgcn_s_waitcnt(vm_enc(QFIRST > 0 ? QFIRST - 1 : 0));
      else if constexpr (N >= PACE) __builtin_amdgcn_s_waitcnt(vm_enc(PACE - 1));
      if constexpr (N >= NA) { constexpr int i = N - NA; vv[i] = ld128<AUX_KV>(rv, vvo, ts[i >> 3] + ((i & 7) >> 1) * 2048 + (i & 1) * 256); }
      else if constexpr (N >= NQ) { constexpr int i = N - NQ; kk[i] = ld128<AUX_KV>(rk, kvo, ts[i >> 3] + ((i & 7) >> 1) * 2048 + (i & 1) * 128); }
      else if constexpr (N < 8) wq[N] = ld128<AUX_WQ>(rq, qvo, qsb + N * (HD * 2));
      else wq[N] = ld128<AUX_WQ>(rq, qvo2, qsb + (32 + N - 8) * (HD * 2));
      SB();
    }
  }
  template <int LO, int HI> __device__ __forceinline__ void issue_range() {
    if constexpr (LO < HI && LO < NALL) { issue<LO>(); issue_range<LO + 1, HI>(); }
  }
};

template <int BR>
__device__ __forceinline__ void body(const Params& p, LAS unsigned char* L, int hg, int j, int w, int lane) {
  using S = Stream<BR>;
  constexpr int NT = S::NT;
  constexpr int NP = BR ? 6 : 2;
  constexpr int J0 = BR ? 0 : 6;
  const int c = lane & 15, g = lane >> 4;
  unsigned tst[16];
#pragma unroll
  for (int i = 0; i < 16; ++i) tst[i] = 0u;
  STAMP(0);
  const unsigned t_start = (unsigned)__builtin_amdgcn_s_memrealtime();
  const __amdgpu_buffer_rsrc_t rws = mk(p.ws, WS_END);
  unsigned e_raw = 0u;
  if (w == 0) e_raw = __builtin_amdgcn_raw_buffer_load_b32(rws, 0, WS_E + 256 * BR, AUX_SC1);

  S s;
  bool has3 = false;
  constexpr int HEAD_BYTES = (BR ? SK : WIN) * 256;
  if (BR) {
    const int wi = j * 8 + w;
    const int n3 = j < 2 ? 6 : 5;
    has3 = w < n3;
    s.ts[0] = wi * 8192; s.ts[1] = (48 + wi) * 8192;
    s.ts[2] = has3 ? (96 + (j < 2 ? 6 * j : 12 + 5 * (j - 2)) + w) * 8192 : HEAD_BYTES;
  } else {
    const int wi = (j - 6) * 8 + w;
    s.ts[0] = wi * 8192; s.ts[1] = (16 + wi) * 8192; s.ts[2] = 0;
  }
  int cb = -1, ru = -1;
  if (BR) {
    if (j < 2) { if (w == 7) cb = j; if (w == 6) ru = j == 0 ? hg : ((hg & 3) == 0 ? 32 + (hg >> 2) : -1); }
    else if (w >= 6) cb = 2 + 2 * (j - 2) + (w - 6);
  } else {
    if (w >= 3) cb = 5 * (j - 6) + (w - 3);
    if (w == 2) ru = j == 6 ? hg : ((hg & 3) == 0 ? 32 + (hg >> 2) : -1);
  }
  const size_t kvoff = ((size_t)hg * SK + (BR ? 0 : SK - WIN)) * 256;
  s.rk = mk(p.k[BR] + kvoff, HEAD_BYTES); s.rv = mk(p.v[BR] + kvoff, HEAD_BYTES);
  s.rq = mk(p.wq[BR], DM * HD * 2);
  const __amdgpu_buffer_rsrc_t rx = mk(p.x[BR], DM * 2);
  const int row0 = BR ? 16 * (6 * w + j) : 40 * ((j - 6) * 8 + w);
  s.qsb = row0 * (HD * 2) + hg * 256;
  s.qvo = g * (BR ? 4 : 8) * (HD * 2) + c * 16; s.qvo2 = g * 2 * (HD * 2) + c * 16;
  s.kvo = (c >> 1) * 256 + (c & 1) * 64 + g * 16;
  s.vvo = g * 512 + c * 16;

  v4u xa0; v2u xa2;
  if (BR) xa2 = __builtin_amdgcn_raw_buffer_load_b64(rx, g * 8, row0 * 2, 0);
  else { xa0 = ld128<0>(rx, g * 16, row0 * 2); xa2 = v2u{__builtin_amdgcn_raw_buffer_load_b32(rx, g * 4, row0 * 2 + 64, 0), 0u}; }
  SB();
  constexpr int P0 = S::NQ + PACE, PP = 4 + POLLPOS, PC = PP + PACE + 1, P2 = S::NA + B1POS;
  static_assert(P0 <= PP && PC <= P2, "order");
  s.template issue_range<0, P0>();
  STAMP(1);
  {
    f32x4 acc[8];
#pragma unroll
    for (int i = 0; i < 8; ++i) acc[i] = f32x4{0.f, 0.f, 0.f, 0.f};
    if constexpr (BR) rows4_mfma(s.wq, xa2, acc);
    else {
      rows8_mfma(s.wq, xa0, acc);
#pragma unroll
      for (int d = 0; d < 4; ++d) {
        acc[2 * d] = mfma16(xa2, v2u{pk_lo(s.wq[8][d], s.wq[9][d]), 0u}, acc[2 * d]);
        acc[2 * d + 1] = mfma16(xa2, v2u{pk_hi(s.wq[8][d], s.wq[9][d]), 0u}, acc[2 * d + 1]);
      }
    }
    if (g == 0) {
      LAS f32x4* qp = (LAS f32x4*)(L + L_QP + w * 512 + c * 32);
      qp[0] = f32x4{acc[0][0], acc[1][0], acc[2][0], acc[3][0]};
      qp[1] = f32x4{acc[4][0], acc[5][0], acc[6][0], acc[7][0]};
    }
  }
  if (w == 0 && lane == 0) *(LAS unsigned*)(L + L_TAG) = e_raw + 1u;
  WG_BARRIER();
  const unsigned tag = __builtin_amdgcn_readfirstlane(*(LAS unsigned*)(L + L_TAG));
  if (w == 7) {
    f32x2 sm = {0.f, 0.f};
#pragma unroll
    for (int ww = 0; ww < 8; ++ww) sm += *(LAS f32x2*)(L + L_QP + ww * 512 + lane * 8);
    const v4u u = {__float_as_uint(sm[0]), tag, __float_as_uint(sm[1]), tag};
    __builtin_amdgcn_raw_buffer_store_b128(u, rws, lane * 16, WS_QP + (hg * 8 + j) * 1024, AUX_SC1);
  }
  SB();
  STAMP(13);
  s.template issue_range<P0, PP>();
  v4u qq[NP];
  if (w == 7) {
#pragma unroll
    for (int q = 0; q < NP; ++q) qq[q] = ld128<AUX_SC1>(rws, lane * 16, WS_QP + (hg * 8 + J0 + q) * 1024);
  }
  SB();
  s.template issue_range<PP, PC>();
  if (w == 7) {
    bool ok = true;
#pragma unroll
    for (int q = 0; q < NP; ++q) ok = ok && qq[q][1] == tag && qq[q][3] == tag;
    if (!__all(ok)) {
      for (unsigned spins = 0;; ++spins) {
      asm volatile("" ::: "memory");
#pragma unroll
        for (int q = 0; q < NP; ++q) qq[q] = ld128<AUX_SC1>(rws, lane * 16, WS_QP + (hg * 8 + J0 + q) * 1024);
        ok = true;
#pragma unroll
        for (int q = 0; q < NP; ++q) ok = ok && qq[q][1] == tag && qq[q][3] == tag;
        if (__all(ok) || spins > SPIN_CAP) break;
      }
    }
    float q0 = 0.f, q1 = 0.f;
#pragma unroll
    for (int q = 0; q < NP; ++q) { q0 += __uint_as_float(qq[q][0]); q1 += __uint_as_float(qq[q][2]); }
    *(LAS unsigned*)(L + L_Q + lane * 4) = cvtpk(q0, q1);
    STAMP(14);
  }
  SB();
  s.template issue_range<PC, P2 + 1>();
  STAMP(2);
  WG_BARRIER();
  const v4u qf0 = *(LAS v4u*)(L + L_Q + (c & 1) * 64 + g * 16);
  const v4u qf1 = *(LAS v4u*)(L + L_Q + 128 + (c & 1) * 64 + g * 16);
  SB();
  s.template issue<P2 + 1>();
  STAMP(3);
  float sc[8 * NT];
  constexpr int PK = S::pos_k(S::NK - 1) + PACE + 1 > P2 + 2 + NT ? S::pos_k(S::NK - 1) + PACE + 1 : P2 + 2 + NT;
#pragma unroll
  for (int T = 0; T < NT; ++T) {
    if (T == 0) s.template issue<P2 + 2>();
    if (T == 1) { if (NT == 2) s.template issue_range<P2 + 3, PK>(); else s.template issue<P2 + 3>(); }
    if (T == 2) s.template issue_range<P2 + 4, PK>();
#pragma unroll
    for (int t = 4 * T; t < 4 * T + 4; ++t) {
      f32x4 d = {0.f, 0.f, 0.f, 0.f};
      d = mfma32(s.kk[2 * t], qf0, d);
      d = mfma32(s.kk[2 * t + 1], qf1, d);
      const float s0 = d[0] + dpp<0xB1>(d[1]), s1 = d[2] + dpp<0xB1>(d[3]);
      const unsigned u = cvtpk(s0, s1);
      const unsigned u2 = cvtpk(bflo(u) * 0.08838834764831845f, bfhi(u) * 0.08838834764831845f);
      sc[2 * t] = bflo(u2);
      sc[2 * t + 1] = bfhi(u2);
    }
    SB();
  }
  if (BR && !has3) {
#pragma unroll
    for (int i = 16; i < 8 * NT; ++i) sc[i] = -INFINITY;
  }
  float mx = sc[0];
#pragma unroll
  for (int i = 1; i < 8 * NT; ++i) mx = fmaxf(mx, sc[i]);
  mx = xrow_max(mx);
  if (lane == 0) *(LAS float*)(L + L_WM + w * 4) = mx;
  SB();
  s.template issue<PK>();
  STAMP(4);
  WG_BARRIER();
  float M;
  {
    const f32x4 a = *(LAS f32x4*)(L + L_WM), b = *(LAS f32x4*)(L + L_WM + 16);
    M = fmaxf(fmaxf(fmaxf(a[0], a[1]), fmaxf(a[2], a[3])), fmaxf(fmaxf(b[0], b[1]), fmaxf(b[2], b[3])));
  }
  SB();
  s.template issue<PK + 1>();
  float lsum = 0.f;
  v4u pa[NT];
#pragma unroll
  for (int T = 0; T < NT; ++T) {
#pragma unroll
    for (int t = 0; t < 4; ++t) {
      const float p0 = __builtin_amdgcn_exp2f((sc[8 * T + 2 * t] - M) * 1.4426950408889634f);
      const float p1 = __builtin_amdgcn_exp2f((sc[8 * T + 2 * t + 1] - M) * 1.4426950408889634f);
      lsum += p0; lsum += p1;
      pa[T][t] = cvtpk(p0, p1);
    }
  }
  lsum = xrow_sum(lsum);
  if (lane == 0) {
    const v4u u = {__float_as_uint(M), tag, __float_as_uint(lsum), tag};
    __builtin_amdgcn_raw_buffer_store_b128(u, rws, 0, WS_ML + hg * 1024 + (j * 8 + w) * 16, AUX_SC1);
  }
  SB();
  STAMP(5);
  constexpr int PV0 = S::pos_v(7) + PACE + 1 > PK + 2 ? S::pos_v(7) + PACE + 1 : PK + 2;
  constexpr int PV1 = S::pos_v(15) + PACE + 1 > PV0 ? S::pos_v(15) + PACE + 1 : PV0;
  f32x4 acc[8];
#pragma unroll
  for (int i = 0; i < 8; ++i) acc[i] = f32x4{0.f, 0.f, 0.f, 0.f};
  s.template issue_range<PK + 2, PV0>();
  rows8_mfma(s.vv, pa[0], acc);
  SB();
  if (NT == 3) {
    s.template issue_range<PV0, PV1>();
    rows8_mfma(s.vv + 8, pa[1], acc);
    SB();
    s.template issue_range<PV1, S::NALL>();
  } else {
    s.template issue_range<PV0, S::NALL>();
  }
  STAMP(6);
  WG_BARRIER();
#define WRITE_OP() do { if (g == 0) { LAS f32x4* op = (LAS f32x4*)(L + L_OP + w * 512 + c * 32); \
    op[0] = f32x4{acc[0][0], acc[1][0], acc[2][0], acc[3][0]}; op[1] = f32x4{acc[4][0], acc[5][0], acc[6][0], acc[7][0]}; } } while (0)
  if (cb < 0) {
    rows8_mfma(s.vv + 8 * (NT - 1), pa[NT - 1], acc);
    WRITE_OP();
    WG_BARRIER();
    STAMP(7);
  }
  if (w == 0) {
    f32x2 sm = {0.f, 0.f};
#pragma unroll
    for (int ww = 0; ww < 8; ++ww) sm += *(LAS f32x2*)(L + L_OP + ww * 512 + lane * 8);
    const v4u u = {__float_as_uint(sm[0]), tag, __float_as_uint(sm[1]), tag};
    __builtin_amdgcn_raw_buffer_store_b128(u, rws, lane * 16, WS_PO + (hg * 8 + j) * 1024, AUX_SC1);
  } else if (cb >= 0) {
    v4u wo[16], ml;
    const int mlv = (lane < NP * 8 ? lane : 0) * 16;
    {
      const __amdgpu_buffer_rsrc_t ro = mk(p.wo[BR], HD * DM * 2);
      const int wovo = ((c >> 3) * 32 + g * 8) * (DM * 2) + (c & 7) * 16;
      const int wosb = hg * 128 * (DM * 2) + cb * 128;
      ml = ld128<AUX_SC1>(rws, mlv, WS_ML + hg * 1024 + J0 * 128);
      constexpr int W0 = BR ? 16 : (WODELAY ? 0 : 4);
#pragma unroll
      for (int i = 0; i < W0; ++i) wo[i] = ld128<AUX_WO>(ro, wovo, wosb + ((i >> 3) * 64 + (i & 7)) * (DM * 2));
      SB();
      if (!BR) rows8_mfma(s.vv + 8 * (NT - 1), pa[NT - 1], acc);
      WRITE_OP();
      WG_BARRIER();
      STAMP(7);
      if (!BR && WODELAY) { while ((unsigned)__builtin_amdgcn_s_memrealtime() - t_start < (unsigned)WODELAY) __builtin_amdgcn_s_sleep(2); }
#pragma unroll
      for (int i = W0; i < 16; ++i) wo[i] = ld128<AUX_WO>(ro, wovo, wosb + ((i >> 3) * 64 + (i & 7)) * (DM * 2));
      SB();
    }
    if (!__all(ml[1] == tag && ml[3] == tag)) {
      for (unsigned spins = 0;; ++spins) {
        asm volatile("" ::: "memory");
        ml = ld128<AUX_SC1>(rws, mlv, WS_ML + hg * 1024 + J0 * 128);
        if (__all(ml[1] == tag && ml[3] == tag) || spins > SPIN_CAP) break;
        __builtin_amdgcn_s_sleep(2);
      }
    }
    float fq[NP];
    {
      const float mj = __uint_as_float(ml[0]);
      float Mg = fmaxf(mj, dpp<0x128>(mj));
      Mg = xrow_max(Mg);
      const float e = __builtin_amdgcn_exp2f((mj - Mg) * 1.4426950408889634f);
      float t = lane < NP * 8 ? e * __uint_as_float(ml[2]) : 0.f;
      t += dpp<0xB1>(t); t += dpp<0x4E>(t); t += dpp<0x124>(t); t += dpp<0x128>(t);
      t = xrow_sum(t);
      const float f = e / t;
#pragma unroll
      for (int q = 0; q < NP; ++q) fq[q] = __uint_as_float(__builtin_amdgcn_readlane(__float_as_uint(f), 8 * q));
    }
    v4u wf[16];
#pragma unroll
    for (int t = 0; t < 2; ++t)
#pragma unroll
      for (int d = 0; d < 4; ++d) {
        const v4u* r = wo + 8 * t;
        wf[8 * t + 2 * d] = v4u{pk_lo(r[0][d], r[1][d]), pk_lo(r[2][d], r[3][d]), pk_lo(r[4][d], r[5][d]), pk_lo(r[6][d], r[7][d])};
        wf[8 * t + 2 * d + 1] = v4u{pk_hi(r[0][d], r[1][d]), pk_hi(r[2][d], r[3][d]), pk_hi(r[4][d], r[5][d]), pk_hi(r[6][d], r[7][d])};
      }
#pragma unroll
    for (int i = 0; i < 16; ++i) asm volatile("" : "+v"(wf[i]));
    STAMP(12);
    v4u po[NP];
    for (unsigned spins = 0;; ++spins) {
      asm volatile("" ::: "memory");
#pragma unroll
      for (int q = 0; q < NP; ++q) po[q] = ld128<AUX_SC1>(rws, lane * 16, WS_PO + (hg * 8 + J0 + q) * 1024);
      bool ok = true;
#pragma unroll
      for (int q = 0; q < NP; ++q) ok = ok && po[q][1] == tag && po[q][3] == tag;
      if (__all(ok) || spins > SPIN_CAP) break;
      __builtin_amdgcn_s_sleep(1);
    }
    STAMP(8);
    float o0 = 0.f, o1 = 0.f;
#pragma unroll
    for (int q = 0; q < NP; ++q) {
      o0 = fmaf(fq[q], __uint_as_float(po[q][0]), o0);
      o1 = fmaf(fq[q], __uint_as_float(po[q][2]), o1);
    }
    *(LAS unsigned*)(L + L_XB + w * 256 + lane * 4) = cvtpk(o0, o1);
    LDS_WAIT();
    const v4u oa0 = *(LAS v4u*)(L + L_XB + w * 256 + (c & 1) * 64 + g * 16);
    const v4u oa1 = *(LAS v4u*)(L + L_XB + w * 256 + 128 + (c & 1) * 64 + g * 16);
    float y[8];
#pragma unroll
    for (int jj = 0; jj < 8; ++jj) {
      f32x4 d = {0.f, 0.f, 0.f, 0.f};
      d = mfma32(oa0, wf[jj], d);
      d = mfma32(oa1, wf[8 + jj], d);
      y[jj] = d[0] + dpp<0x128>(d[1]);
    }
    const float y0 = g == 0 ? y[0] : g == 1 ? y[2] : g == 2 ? y[4] : y[6];
    const float y1 = g == 0 ? y[1] : g == 1 ? y[3] : g == 2 ? y[5] : y[7];
    const v4u u = {__float_as_uint(y0), tag, __float_as_uint(y1), tag};
    if (c < 8) __builtin_amdgcn_raw_buffer_store_b128(u, rws, c * 64 + g * 16, WS_Y + ((BR * 10 + cb) * 32 + hg) * 512, AUX_SC1);
    STAMP(9);
  } else if (ru >= 0) {
    v4u yy[4];
    const int vo = (lane >> 3) * 512 + (lane & 7) * 16;
    const int so = WS_Y + (BR * 10 + (ru >> 2)) * 32 * 512 + (ru & 3) * 128;
    for (unsigned spins = 0;; ++spins) {
      asm volatile("" ::: "memory");
#pragma unroll
      for (int i = 0; i < 4; ++i) yy[i] = ld128<AUX_SC1>(rws, vo, so + i * 8 * 512);
      bool ok = true;
#pragma unroll
      for (int i = 0; i < 4; ++i) ok = ok && yy[i][1] == tag && yy[i][3] == tag;
      if (__all(ok) || spins > SPIN_CAP) break;
      __builtin_amdgcn_s_sleep(1);
    }
    STAMP(10);
    float a0 = (__uint_as_float(yy[0][0]) + __uint_as_float(yy[1][0])) + (__uint_as_float(yy[2][0]) + __uint_as_float(yy[3][0]));
    float a1 = (__uint_as_float(yy[0][2]) + __uint_as_float(yy[1][2])) + (__uint_as_float(yy[2][2]) + __uint_as_float(yy[3][2]));
    a0 += dpp<0x128>(a0); a1 += dpp<0x128>(a1);
    a0 = xrow_sum(a0); a1 = xrow_sum(a1);
    if (lane < 8) *(unsigned*)(p.out + (BR * DM + ru * 16 + lane * 2) * 2) = cvtpk(a0, a1);
    if (ru == 0 && lane == 0) __builtin_amdgcn_raw_buffer_store_b32(tag, rws, 0, WS_E + 256 * BR, AUX_SC1);
    STAMP(11);
  }
}

__global__ void __launch_bounds__(512, 2) fused_decode_kernel(Params p) {
  __shared__ __attribute__((aligned(16))) unsigned char smem[L_END];
  LAS unsigned char* L = (LAS unsigned char*)smem;
  const int b = blockIdx.x, lane = threadIdx.x & 63, w = __builtin_amdgcn_readfirstlane(threadIdx.x >> 6);
  const int hg = (b & 7) * 4 + (b >> 6), j = (b >> 3) & 7;
  if (j < 6) body<1>(p, L, hg, j, w, lane);
  else body<0>(p, L, hg, j, w, lane);
}

extern "C" void kernel_launch(void* const* d_in, const int* in_sizes, int n_in, void* d_out, int out_size, void* d_ws, size_t ws_size,
                              hipStream_t stream) {
  Params p;
  for (int br = 0; br < 2; ++br) {
    p.x[br] = (const unsigned char*)d_in[5 * br + 0];
    p.k[br] = (const unsigned char*)d_in[5 * br + 1];
    p.v[br] = (const unsigned char*)d_in[5 * br + 2];
    p.wq[br] = (const unsigned char*)d_in[5 * br + 3];
    p.wo[br] = (const unsigned char*)d_in[5 * br + 4];
  }
  p.out = (unsigned char*)d_out;
  p.ws = (unsigned char*)d_ws;
  fused_decode_kernel<<<256, 512, 0, stream>>>(p);
}
```

```cpp
#include <hip/hip_runtime.h>
#include <stdint.h>

constexpr int PACEB = 5, PACEA = 5;
constexpr int TAUB = 480, TAUA = 465;
constexpr int POLLPOS = 14, B1POS = 2;
constexpr int NAB = 6;

typedef unsigned v4u __attribute__((ext_vector_type(4)));
typedef unsigned v2u __attribute__((ext_vector_type(2)));
typedef float f32x4 __attribute__((ext_vector_type(4)));
typedef float f32x2 __attribute__((ext_vector_type(2)));
typedef __bf16 bf16x8 __attribute__((ext_vector_type(8)));
typedef __bf16 bf16x2 __attribute__((ext_vector_type(2)));
typedef short s16x4 __attribute__((ext_vector_type(4)));
#define LAS __attribute__((address_space(3)))
#define GAS __attribute__((address_space(1)))

constexpr int DM = 640, NH = 32, SK = 4096, DH = 128, WIN = 1024, HD = NH * DH;
constexpr int AUX_NT = 2, AUX_SC1 = 16;
constexpr unsigned RSRC_W3 = 0x00020000u;
constexpr float SCALE = 0.08838834764831845f, LOG2E = 1.4426950408889634f;
constexpr int WS_E = 0;
constexpr int WS_ML = 4096;
constexpr int WS_PO = WS_ML + 32 * 1024;
constexpr int WS_Y = WS_PO + 32 * 8 * 1024;
constexpr int WS_QP = WS_Y + 2 * 10 * 32 * 512;
constexpr int WS_END = WS_QP + 32 * 8 * 1024;
constexpr unsigned SPIN_CAP = 1u << 16;
constexpr int L_QP = 0;
constexpr int L_OP = 4096;
constexpr int L_XB = 8192;
constexpr int L_WM = 10240;
constexpr int L_Q = 10272;
constexpr int L_TAG = 10528;
constexpr int L_ZERO = 10544;

struct Params {
  const unsigned char* x[2];
  const unsigned char* k[2];
  const unsigned char* v[2];
  const unsigned char* wq[2];
  const unsigned char* wo[2];
  unsigned char* out;
  unsigned char* ws;
};

__device__ __forceinline__ __amdgpu_buffer_rsrc_t mk(const unsigned char* p, unsigned bytes) {
  return __builtin_amdgcn_make_buffer_rsrc((void*)p, 0, (int)bytes, RSRC_W3);
}
template <int AUX> __device__ __forceinline__ v4u ld128(__amdgpu_buffer_rsrc_t rs, int voff, int soff) {
  return __builtin_amdgcn_raw_buffer_load_b128(rs, voff, soff, AUX);
}
__device__ __forceinline__ f32x4 mfma32(v4u a, v4u b, f32x4 c) {
  return __builtin_amdgcn_mfma_f32_16x16x32_bf16(__builtin_bit_cast(bf16x8, a), __builtin_bit_cast(bf16x8, b), c, 0, 0, 0);
}
__device__ __forceinline__ f32x4 mfma16(v2u a, v2u b, f32x4 c) {
  return __builtin_amdgcn_mfma_f32_16x16x16bf16_1k(__builtin_bit_cast(s16x4, a), __builtin_bit_cast(s16x4, b), c, 0, 0, 0);
}
__device__ __forceinline__ unsigned pk_lo(unsigned r0, unsigned r1) { return __builtin_amdgcn_perm(r1, r0, 0x05040100u); }
__device__ __forceinline__ unsigned pk_hi(unsigned r0, unsigned r1) { return __builtin_amdgcn_perm(r1, r0, 0x07060302u); }
__device__ __forceinline__ unsigned cvtpk(float lo, float hi) {
  f32x2 f = {lo, hi};
  return __builtin_bit_cast(unsigned, __builtin_convertvector(f, bf16x2));
}
__device__ __forceinline__ float bflo(unsigned u) { return __builtin_bit_cast(float, u << 16); }
__device__ __forceinline__ float bfhi(unsigned u) { return __builtin_bit_cast(float, u & 0xffff0000u); }
__device__ __forceinline__ float ex2(float x) { return __builtin_amdgcn_exp2f(x); }
template <int CTRL> __device__ __forceinline__ float dpp(float x) {
  return __builtin_bit_cast(float, __builtin_amdgcn_mov_dpp(__builtin_bit_cast(int, x), CTRL, 0xf, 0xf, true));
}
__device__ __forceinline__ float xrow_max(float x) {
  auto s = __builtin_amdgcn_permlane16_swap(__float_as_uint(x), __float_as_uint(x), false, false);
  x = fmaxf(__uint_as_float(s[0]), __uint_as_float(s[1]));
  auto t = __builtin_amdgcn_permlane32_swap(__float_as_uint(x), __float_as_uint(x), false, false);
  return fmaxf(__uint_as_float(t[0]), __uint_as_float(t[1]));
}
__device__ __forceinline__ float xrow_sum(float x) {
  auto s = __builtin_amdgcn_permlane16_swap(__float_as_uint(x), __float_as_uint(x), false, false);
  x = __uint_as_float(s[0]) + __uint_as_float(s[1]);
  auto t = __builtin_amdgcn_permlane32_swap(__float_as_uint(x), __float_as_uint(x), false, false);
  return __uint_as_float(t[0]) + __uint_as_float(t[1]);
}
__device__ __forceinline__ bool tagged(const v4u& u, unsigned tag) { return u[1] == tag && u[3] == tag; }
constexpr int vm_enc(int n) { return (n & 0xF) | ((n >> 4) << 14) | (0x7 << 4) | (0xF << 8); }
constexpr int imax(int a, int b) { return a > b ? a : b; }
#define SB() __builtin_amdgcn_sched_barrier(0)
#define REREAD() asm volatile("" ::: "memory")
#define WG_BARRIER() do { SB(); asm volatile("s_waitcnt lgkmcnt(0)\n\ts_barrier" ::: "memory"); SB(); } while (0)

__device__ __forceinline__ void rows8_mfma(const v4u* r, v4u a, f32x4* acc) {
#pragma unroll
  for (int d = 0; d < 4; ++d) {
    v4u lo = {pk_lo(r[0][d], r[1][d]), pk_lo(r[2][d], r[3][d]), pk_lo(r[4][d], r[5][d]), pk_lo(r[6][d], r[7][d])};
    v4u hi = {pk_hi(r[0][d], r[1][d]), pk_hi(r[2][d], r[3][d]), pk_hi(r[4][d], r[5][d]), pk_hi(r[6][d], r[7][d])};
    acc[2 * d] = mfma32(a, lo, acc[2 * d]);
    acc[2 * d + 1] = mfma32(a, hi, acc[2 * d + 1]);
  }
}
__device__ __forceinline__ void rows4_mfma(const v4u* r, v2u a, f32x4* acc) {
#pragma unroll
  for (int d = 0; d < 4; ++d) {
    v2u lo = {pk_lo(r[0][d], r[1][d]), pk_lo(r[2][d], r[3][d])};
    v2u hi = {pk_hi(r[0][d], r[1][d]), pk_hi(r[2][d], r[3][d])};
    acc[2 * d] = mfma16(a, lo, acc[2 * d]);
    acc[2 * d + 1] = mfma16(a, hi, acc[2 * d + 1]);
  }
}
__device__ __forceinline__ void rows6_mfma(const v4u* r, v4u a, f32x4* acc) {
#pragma unroll
  for (int d = 0; d < 4; ++d) {
    v4u lo = {pk_lo(r[0][d], r[1][d]), pk_lo(r[2][d], r[3][d]), pk_lo(r[4][d], r[5][d]), 0u};
    v4u hi = {pk_hi(r[0][d], r[1][d]), pk_hi(r[2][d], r[3][d]), pk_hi(r[4][d], r[5][d]), 0u};
    acc[2 * d] = mfma32(a, lo, acc[2 * d]);
    acc[2 * d + 1] = mfma32(a, hi, acc[2 * d + 1]);
  }
}
__device__ __forceinline__ void score8(v4u k0, v4u k1, v4u qf0, v4u qf1, float& s0, float& s1) {
  f32x4 d = {0.f, 0.f, 0.f, 0.f};
  d = mfma32(k0, qf0, d);
  d = mfma32(k1, qf1, d);
  const unsigned u = cvtpk(d[0] + dpp<0xB1>(d[1]), d[2] + dpp<0xB1>(d[3]));
  const unsigned u2 = cvtpk(bflo(u) * SCALE, bfhi(u) * SCALE);
  s0 = bflo(u2); s1 = bfhi(u2);
}

template <int BR> struct Stream {
  static constexpr int PACE = BR ? PACEB : PACEA, TAU = BR ? TAUB : TAUA;
  static constexpr int NQ = BR ? 4 : 12;
  static constexpr int NK = BR ? 22 : 16;
  static constexpr int NA = NQ + NK;
  static constexpr int NALL = NA + NK;
  __amdgpu_buffer_rsrc_t rq, rk, rv;
  int qvo, qvo2, qsb, kvo, vvo, ts[4];
  unsigned t0;
  v4u wq[NQ], kk[NK], vv[NK];
  static constexpr int pos_k(int i) { return NQ + i; }
  static constexpr int pos_v(int i) { return NA + i; }
  static constexpr int tsel(int i) { return i < 20 ? i >> 3 : 3; }
  static constexpr int toff(int i) { return i < 20 ? ((i & 7) >> 1) * 2048 : 0; }
  template <int N> __device__ __forceinline__ void issue() {
    if constexpr (N < NALL) {
      constexpr bool timed = TAU > 0 && N >= PACE + 2 && (N & 1) == 0;
      unsigned now = 0u;
      if constexpr (timed) { now = (unsigned)__builtin_amdgcn_s_memrealtime(); SB(); }
      if constexpr (N >= PACE) __builtin_amdgcn_s_waitcnt(vm_enc(PACE - 1));
      if constexpr (timed) {
        SB();
        constexpr unsigned due = (unsigned)((N - PACE) * TAU) >> 4;
        while (now - t0 < due) { __builtin_amdgcn_s_sleep(1); now = (unsigned)__builtin_amdgcn_s_memrealtime(); }
      }
      if constexpr (N >= NA) { constexpr int i = N - NA; vv[i] = ld128<AUX_NT>(rv, vvo, ts[tsel(i)] + toff(i) + (i & 1) * 256); }
      else if constexpr (N >= NQ) { constexpr int i = N - NQ; kk[i] = ld128<AUX_NT>(rk, kvo, ts[tsel(i)] + toff(i) + (i & 1) * 128); }
      else if constexpr (N < 8) wq[N] = ld128<AUX_NT>(rq, qvo, qsb + N * (HD * 2));
      else wq[N] = ld128<AUX_NT>(rq, qvo2, qsb + (32 + N - 8) * (HD * 2));
      SB();
    }
  }
  template <int LO, int HI> __device__ __forceinline__ void issue_range() {
    if constexpr (LO < HI && LO < NALL) { issue<LO>(); issue_range<LO + 1, HI>(); }
  }
};

template <int PB>
__device__ __forceinline__ void project(__amdgpu_buffer_rsrc_t rws, LAS unsigned char* L, const v4u* wo, v4u ml, int hg, int cb, int w, int lane, unsigned tag) {
  constexpr int NP = PB ? 6 : 2, J0 = PB ? 0 : 6;
  const int c = lane & 15, g = lane >> 4;
  const int mlv = (lane < NP * 8 ? lane : 0) * 16;
  const int xb = L_XB + w * 256 + (c & 1) * 64 + g * 16;
  if (!__all(tagged(ml, tag))) {
    for (unsigned spins = 0;; ++spins) {
      REREAD();
      ml = ld128<AUX_SC1>(rws, mlv, WS_ML + hg * 1024 + J0 * 128);
      if (__all(tagged(ml, tag)) || spins > SPIN_CAP) break;
      __builtin_amdgcn_s_sleep(2);
    }
  }
  float fq[NP];
  {
    const float mj = __uint_as_float(ml[0]);
    const float Mg = xrow_max(fmaxf(mj, dpp<0x128>(mj)));
    const float e = ex2((mj - Mg) * LOG2E);
    float t = lane < NP * 8 ? e * __uint_as_float(ml[2]) : 0.f;
    t += dpp<0xB1>(t); t += dpp<0x4E>(t); t += dpp<0x124>(t); t += dpp<0x128>(t);
    const float f = e / xrow_sum(t);
#pragma unroll
    for (int q = 0; q < NP; ++q) fq[q] = __uint_as_float(__builtin_amdgcn_readlane(__float_as_uint(f), 8 * q));
  }
  v4u wf[16];
#pragma unroll
  for (int t = 0; t < 2; ++t)
#pragma unroll
    for (int d = 0; d < 4; ++d) {
      const v4u* r = wo + 8 * t;
      wf[8 * t + 2 * d] = v4u{pk_lo(r[0][d], r[1][d]), pk_lo(r[2][d], r[3][d]), pk_lo(r[4][d], r[5][d]), pk_lo(r[6][d], r[7][d])};
      wf[8 * t + 2 * d + 1] = v4u{pk_hi(r[0][d], r[1][d]), pk_hi(r[2][d], r[3][d]), pk_hi(r[4][d], r[5][d]), pk_hi(r[6][d], r[7][d])};
    }
#pragma unroll
  for (int i = 0; i < 16; ++i) asm volatile("" : "+v"(wf[i]));
  int ax[8], xw = L_XB + w * 256 + lane * 4, yo = c < 8 ? c * 64 + g * 16 : WS_END;
#pragma unroll
  for (int i = 0; i < 8; ++i) { ax[i] = (c >> 2) == (i >> 1) ? xb + (i & 1) * 128 : L_ZERO; asm volatile("" : "+v"(ax[i])); }
  asm volatile("" : "+v"(xw), "+v"(yo));
  v4u po[NP];
  for (unsigned spins = 0;; ++spins) {
    REREAD();
#pragma unroll
    for (int q = 0; q < NP; ++q) po[q] = ld128<AUX_SC1>(rws, lane * 16, WS_PO + (hg * 8 + J0 + q) * 1024);
    bool ok = true;
#pragma unroll
    for (int q = 0; q < NP; ++q) ok = ok && tagged(po[q], tag);
    if (__all(ok) || spins > SPIN_CAP) break;
    __builtin_amdgcn_s_sleep(1);
  }
  float o0 = 0.f, o1 = 0.f;
#pragma unroll
  for (int q = 0; q < NP; ++q) { o0 = fmaf(fq[q], __uint_as_float(po[q][0]), o0); o1 = fmaf(fq[q], __uint_as_float(po[q][2]), o1); }
  *(LAS unsigned*)(L + xw) = cvtpk(o0, o1);
  REREAD();
  v4u oa[8];
#pragma unroll
  for (int i = 0; i < 8; ++i) oa[i] = *(LAS v4u*)(L + ax[i]);
  SB();
  f32x4 d0 = {0.f, 0.f, 0.f, 0.f}, d1 = {0.f, 0.f, 0.f, 0.f};
#pragma unroll
  for (int G = 0; G < 4; ++G) {
    d0 = mfma32(oa[2 * G], wf[2 * G], d0);         d1 = mfma32(oa[2 * G], wf[2 * G + 1], d1);
    d0 = mfma32(oa[2 * G + 1], wf[8 + 2 * G], d0); d1 = mfma32(oa[2 * G + 1], wf[9 + 2 * G], d1);
  }
  const float y0 = d0[0] + dpp<0x128>(d0[1]), y1 = d1[0] + dpp<0x128>(d1[1]);
  const v4u u = {__float_as_uint(y0), tag, __float_as_uint(y1), tag};
  __builtin_amdgcn_raw_buffer_store_b128(u, rws, yo, WS_Y + ((PB * 10 + cb) * 32 + hg) * 512, AUX_SC1);
}

template <int BR>
__device__ __forceinline__ void body(const Params& p, LAS unsigned char* L, int hg, int j, int w, int lane, unsigned long long t0, int row0, int tile0) {
  using S = Stream<BR>;
  constexpr int PACE = S::PACE, NG = S::NK / 2;
  constexpr int NP = BR ? 6 : 2;
  constexpr int J0 = BR ? 0 : 6;
  constexpr int HEAD_BYTES = (BR ? SK : WIN) * 256;
  const int c = lane & 15, g = lane >> 4;
  S s;
  s.t0 = (unsigned)t0;
  const size_t kvoff = ((size_t)hg * SK + (BR ? 0 : SK - WIN)) * 256;
  s.rq = mk(p.wq[BR], DM * HD * 2);
  s.rk = mk(p.k[BR] + kvoff, HEAD_BYTES);
  const __amdgpu_buffer_rsrc_t rx = mk(p.x[BR], DM * 2);
  s.qsb = row0 * (HD * 2) + hg * 256;
  s.qvo = g * (BR ? 4 : 8) * (HD * 2) + c * 16;
  s.kvo = (c >> 1) * 256 + (c & 1) * 64 + g * 16;
  s.ts[0] = tile0 * 8192;
  v4u xa0; v2u xa2;
  if (BR) xa2 = __builtin_amdgcn_raw_buffer_load_b64(rx, g * 8, row0 * 2, 0);
  else { xa0 = ld128<0>(rx, g * 16, row0 * 2); xa2 = __builtin_amdgcn_raw_buffer_load_b64(rx, g * 8, row0 * 2 + 64, 0); }
  SB();
  s.template issue_range<0, PACE>();
  asm volatile("" :: "s"(t0));
  const __amdgpu_buffer_rsrc_t rws = mk(p.ws, WS_END);
  unsigned e_raw = 0u;
  if (w == 0) e_raw = __builtin_amdgcn_raw_buffer_load_b32(rws, 0, WS_E, AUX_SC1);
  bool has3 = true;
  if (BR) {
    const int n2 = j < 3 ? 0 : (j == 3 ? 6 : 5);
    has3 = w >= n2;
    s.ts[1] = s.ts[0] + 48 * 8192;
    s.ts[2] = 96 * 8192 + ((int)((0x6D5A48301800ull >> (8 * j)) & 255) + 3 * w - (w < n2 ? w : n2)) * 2048;
    s.ts[3] = has3 ? s.ts[2] + 4096 : HEAD_BYTES;
  } else {
    s.ts[1] = s.ts[0] + 16 * 8192; s.ts[2] = 0; s.ts[3] = 0;
  }
  s.rv = mk(p.v[BR] + kvoff, HEAD_BYTES);
  s.qvo2 = j == 6 ? g * 4 * (HD * 2) + c * 16 : DM * HD * 2;
  s.vvo = g * 512 + c * 16;
  int pb = 0, cb = -1, ru = -1;
  if (BR) {
    if (w == 7) { pb = 1; cb = j; }
    else if (w == 6) { pb = j < 4; cb = j < 4 ? 6 + j : j - 4; }
    else if (w == 5 && j < NAB - 2) cb = j + 2;
  } else {
    constexpr int PER = (10 - NAB) / 2;
    if (w >= 8 - PER) cb = NAB + (j - 6) * PER + (w - (8 - PER));
    const int a = hg * 2 + (j - 6);
    if (w == 1) ru = a;
    if (w == 2) ru = (a & 3) == 0 ? 64 + (a >> 2) : (a == 1 ? 80 : -1);
  }
  SB();
  constexpr int P0 = S::NQ + PACE, PP = imax(4 + POLLPOS, P0), PC = PP + PACE + 1, P2 = imax(S::NA + B1POS, PC);
  s.template issue_range<PACE, P0>();
  {
    f32x4 acc[8];
#pragma unroll
    for (int i = 0; i < 8; ++i) acc[i] = f32x4{0.f, 0.f, 0.f, 0.f};
    if constexpr (BR) rows4_mfma(s.wq, xa2, acc);
    else { rows8_mfma(s.wq, xa0, acc); rows4_mfma(s.wq + 8, xa2, acc); }
    if (g == 0) {
      LAS f32x4* qp = (LAS f32x4*)(L + L_QP + w * 512 + c * 32);
      qp[0] = f32x4{acc[0][0], acc[1][0], acc[2][0], acc[3][0]};
      qp[1] = f32x4{acc[4][0], acc[5][0], acc[6][0], acc[7][0]};
    }
  }
  asm volatile("" : "+v"(e_raw));
  if (w == 0 && lane == 0) { *(LAS unsigned*)(L + L_TAG) = e_raw + 1u; *(LAS v4u*)(L + L_ZERO) = v4u{0u, 0u, 0u, 0u}; }
  WG_BARRIER();
  const unsigned tag = __builtin_amdgcn_readfirstlane(*(LAS unsigned*)(L + L_TAG));
  if (w == 7) {
    f32x2 sm = {0.f, 0.f};
#pragma unroll
    for (int ww = 0; ww < 8; ++ww) sm += *(LAS f32x2*)(L + L_QP + ww * 512 + lane * 8);
    const v4u u = {__float_as_uint(sm[0]), tag, __float_as_uint(sm[1]), tag};
    __builtin_amdgcn_raw_buffer_store_b128(u, rws, lane * 16, WS_QP + (hg * 8 + j) * 1024, AUX_SC1);
  }
  SB();
  s.template issue_range<P0, PP>();
  v4u qq[NP];
  if (w == 7) {
#pragma unroll
    for (int q = 0; q < NP; ++q) qq[q] = ld128<AUX_SC1>(rws, lane * 16, WS_QP + (hg * 8 + J0 + q) * 1024);
  }
  SB();
  s.template issue_range<PP, PC>();
  if (w == 7) {
    bool ok = true;
#pragma unroll
    for (int q = 0; q < NP; ++q) ok = ok && tagged(qq[q], tag);
    if (!__all(ok)) {
      for (unsigned spins = 0;; ++spins) {
        REREAD();
#pragma unroll
        for (int q = 0; q < NP; ++q) qq[q] = ld128<AUX_SC1>(rws, lane * 16, WS_QP + (hg * 8 + J0 + q) * 1024);
        ok = true;
#pragma unroll
        for (int q = 0; q < NP; ++q) ok = ok && tagged(qq[q], tag);
        if (__all(ok) || spins > SPIN_CAP) break;
      }
    }
    float q0 = 0.f, q1 = 0.f;
#pragma unroll
    for (int q = 0; q < NP; ++q) { q0 += __uint_as_float(qq[q][0]); q1 += __uint_as_float(qq[q][2]); }
    *(LAS unsigned*)(L + L_Q + lane * 4) = cvtpk(q0, q1);
  }
  SB();
  s.template issue_range<PC, P2 + 1>();
  WG_BARRIER();
  const v4u qf0 = *(LAS v4u*)(L + L_Q + (c & 1) * 64 + g * 16);
  const v4u qf1 = *(LAS v4u*)(L + L_Q + 128 + (c & 1) * 64 + g * 16);
  SB();
  s.template issue<P2 + 1>();
  float sc[S::NK];
  constexpr int PK = imax(S::pos_k(S::NK - 1) + PACE + 1, P2 + 5);
  s.template issue<P2 + 2>();
#pragma unroll
  for (int t = 0; t < 4; ++t) score8(s.kk[2 * t], s.kk[2 * t + 1], qf0, qf1, sc[2 * t], sc[2 * t + 1]);
  SB();
  s.template issue<P2 + 3>();
#pragma unroll
  for (int t = 4; t < (BR ? 8 : 6); ++t) score8(s.kk[2 * t], s.kk[2 * t + 1], qf0, qf1, sc[2 * t], sc[2 * t + 1]);
  SB();
  s.template issue_range<P2 + 4, PK>();
#pragma unroll
  for (int t = (BR ? 8 : 6); t < NG; ++t) score8(s.kk[2 * t], s.kk[2 * t + 1], qf0, qf1, sc[2 * t], sc[2 * t + 1]);
  if (BR && !has3) { sc[S::NK - 2] = -INFINITY; sc[S::NK - 1] = -INFINITY; }
  float mx = sc[0];
#pragma unroll
  for (int i = 1; i < S::NK; ++i) mx = fmaxf(mx, sc[i]);
  mx = xrow_max(mx);
  if (lane == 0) *(LAS float*)(L + L_WM + w * 4) = mx;
  SB();
  s.template issue<PK>();
  WG_BARRIER();
  float M;
  {
    const f32x4 a = *(LAS f32x4*)(L + L_WM), b = *(LAS f32x4*)(L + L_WM + 16);
    M = fmaxf(fmaxf(fmaxf(a[0], a[1]), fmaxf(a[2], a[3])), fmaxf(fmaxf(b[0], b[1]), fmaxf(b[2], b[3])));
  }
  SB();
  s.template issue<PK + 1>();
  float lsum = 0.f;
  unsigned pk[NG];
#pragma unroll
  for (int t = 0; t < NG; ++t) {
    const float p0 = ex2((sc[2 * t] - M) * LOG2E), p1 = ex2((sc[2 * t + 1] - M) * LOG2E);
    lsum += p0; lsum += p1;
    pk[t] = cvtpk(p0, p1);
  }
  lsum = xrow_sum(lsum);
  if (lane == 0) {
    const v4u u = {__float_as_uint(M), tag, __float_as_uint(lsum), tag};
    __builtin_amdgcn_raw_buffer_store_b128(u, rws, 0, WS_ML + hg * 1024 + (j * 8 + w) * 16, AUX_SC1);
  }
  SB();
  constexpr int PV0 = imax(S::pos_v(7) + PACE + 1, PK + 2), PV1 = imax(S::pos_v(15) + PACE + 1, PV0);
  f32x4 acc[8];
#pragma unroll
  for (int i = 0; i < 8; ++i) acc[i] = f32x4{0.f, 0.f, 0.f, 0.f};
  s.template issue_range<PK + 2, PV0>();
  rows8_mfma(s.vv, v4u{pk[0], pk[1], pk[2], pk[3]}, acc);
  SB();
  if constexpr (BR) {
    s.template issue_range<PV0, PV1>();
    rows8_mfma(s.vv + 8, v4u{pk[4], pk[5], pk[6], pk[7]}, acc);
    SB();
    s.template issue_range<PV1, S::NALL>();
  } else {
    s.template issue_range<PV0, S::NALL>();
  }
  WG_BARRIER();
#define LAST_PV() do { \
    if constexpr (BR) rows6_mfma(s.vv + 16, v4u{pk[NG - 3], pk[NG - 2], pk[NG - 1], 0u}, acc); else rows8_mfma(s.vv + 8, v4u{pk[4], pk[5], pk[6], pk[7]}, acc); \
    if (g == 0) { LAS f32x4* op = (LAS f32x4*)(L + L_OP + w * 512 + c * 32); \
      op[0] = f32x4{acc[0][0], acc[1][0], acc[2][0], acc[3][0]}; op[1] = f32x4{acc[4][0], acc[5][0], acc[6][0], acc[7][0]}; } \
    WG_BARRIER(); } while (0)
  if (cb >= 0) {
    v4u wo[16];
    const __amdgpu_buffer_rsrc_t ro = mk(pb ? p.wo[1] : p.wo[0], HD * DM * 2);
    const int wovo = ((c >> 3) * 32 + g * 8) * (DM * 2) + (c & 7) * 16;
    const int wosb = hg * 128 * (DM * 2) + cb * 128;
    const v4u ml = ld128<AUX_SC1>(rws, (lane < (pb ? 48 : 16) ? lane : 0) * 16, WS_ML + hg * 1024 + (pb ? 0 : 768));
#pragma unroll
    for (int i = 0; i < 16; ++i) wo[i] = ld128<AUX_NT>(ro, wovo, wosb + ((i >> 3) * 64 + (i & 7)) * (DM * 2));
    SB();
    LAST_PV();
    if (BR && pb) project<1>(rws, L, wo, ml, hg, cb, w, lane, tag);
    else project<0>(rws, L, wo, ml, hg, cb, w, lane, tag);
  } else {
    LAST_PV();
    if (w == 0) {
      f32x2 sm = {0.f, 0.f};
#pragma unroll
      for (int ww = 0; ww < 8; ++ww) sm += *(LAS f32x2*)(L + L_OP + ww * 512 + lane * 8);
      const v4u u = {__float_as_uint(sm[0]), tag, __float_as_uint(sm[1]), tag};
      __builtin_amdgcn_raw_buffer_store_b128(u, rws, lane * 16, WS_PO + (hg * 8 + j) * 1024, AUX_SC1);
    } else if (!BR && ru >= 0) {
      const bool keeper = ru == 80;
      const int vo = keeper ? (lane >> 5) * (10 * 32 * 512) + (lane & 31) * 512 : (lane >> 3) * 512 + (lane & 7) * 16;
      const int so = keeper ? WS_Y : WS_Y + (ru >> 2) * 32 * 512 + (ru & 3) * 128;
      const int st = keeper ? 0 : 8 * 512;
      unsigned pf = 0u;
      if (lane == 0) pf = __builtin_nontemporal_load((const GAS unsigned*)(p.out + (keeper ? 0 : ru * 32)));
      for (unsigned spins = 0; spins < SPIN_CAP; ++spins) {
        REREAD();
        const v4u y1 = ld128<AUX_SC1>(rws, 0, WS_PO + hg * 8 * 1024);
        if (__all(tagged(y1, tag))) break;
        __builtin_amdgcn_s_sleep(4);
      }
      asm volatile("" :: "v"(pf));
      v4u yy[4];
      for (unsigned spins = 0;; ++spins) {
        REREAD();
#pragma unroll
        for (int i = 0; i < 4; ++i) yy[i] = ld128<AUX_SC1>(rws, vo, so + i * st);
        bool ok = true;
#pragma unroll
        for (int i = 0; i < 4; ++i) ok = ok && tagged(yy[i], tag);
        if (__all(ok) || spins > SPIN_CAP) break;
        __builtin_amdgcn_s_sleep(1);
      }
      if (keeper) {
        if (lane == 0) __builtin_amdgcn_raw_buffer_store_b32(tag, rws, 0, WS_E, AUX_SC1);
      } else {
        float a0 = (__uint_as_float(yy[0][0]) + __uint_as_float(yy[1][0])) + (__uint_as_float(yy[2][0]) + __uint_as_float(yy[3][0]));
        float a1 = (__uint_as_float(yy[0][2]) + __uint_as_float(yy[1][2])) + (__uint_as_float(yy[2][2]) + __uint_as_float(yy[3][2]));
        a0 += dpp<0x128>(a0); a1 += dpp<0x128>(a1);
        a0 = xrow_sum(a0); a1 = xrow_sum(a1);
        if (lane < 8) __builtin_amdgcn_raw_buffer_store_b32(cvtpk(a0, a1), mk(p.out, 2 * DM * 2), lane * 4, ru * 32, AUX_SC1);
      }
    }
  }
}

__global__ void __launch_bounds__(512, 2) fused_decode_kernel(Params p) {
  __shared__ __attribute__((aligned(16))) unsigned char smem[82 * 1024];
  LAS unsigned char* L = (LAS unsigned char*)smem;
  const unsigned long long t0 = __builtin_amdgcn_s_memrealtime();
  const int b = blockIdx.x, lane = threadIdx.x & 63, w = __builtin_amdgcn_readfirstlane(threadIdx.x >> 6);
  const int hg = ((b >> 1) & 3) * 8 + (b >> 5), m4 = (b >> 3) & 3, odd = b & 1;
  const int isa = (m4 + 1) >> 2;
  const int j = m4 + 3 * odd + isa * (3 - 2 * odd);
  const int nu = (0x88556888u >> (4 * j)) & 15;
  const int rstep = 16 + isa * (32 - 16 * odd);
  const int rbase = (int)((0x1800231E18100800ull >> (8 * j)) & 255) * 16;
  const int row0 = rbase + rstep * w + ((nu - 1 - w) >> 31 & DM);
  const int tile0 = (j - 6 * isa) * 8 + w;
  if (!isa) body<1>(p, L, hg, j, w, lane, t0, row0, tile0);
  else body<0>(p, L, hg, j, w, lane, t0, row0, tile0);
}

extern "C" void kernel_launch(void* const* d_in, const int* in_sizes, int n_in, void* d_out, int out_size, void* d_ws, size_t ws_size,
                              hipStream_t stream) {
  Params p;
  for (int br = 0; br < 2; ++br) {
    p.x[br] = (const unsigned char*)d_in[5 * br + 0];
    p.k[br] = (const unsigned char*)d_in[5 * br + 1];
    p.v[br] = (const unsigned char*)d_in[5 * br + 2];
    p.wq[br] = (const unsigned char*)d_in[5 * br + 3];
    p.wo[br] = (const unsigned char*)d_in[5 * br + 4];
  }
  p.out = (unsigned char*)d_out;
  p.ws = (unsigned char*)d_ws;
  fused_decode_kernel<<<256, 512, 0, stream>>>(p);
}
```
